```python
import torch

def _softmax(x, axis=-1):
    return torch.softmax(x, dim=axis)
def _gelu(x):
    return torch.nn.functional.gelu(x, approximate="tanh")
def _silu(x):
    return torch.nn.functional.silu(x)
def _sigmoid(x):
    return torch.sigmoid(x)
def _layernorm(x, axis=-1, eps=1e-5):
    m = x.mean(dim=axis, keepdim=True)
    v = x.var(dim=axis, keepdim=True, unbiased=False)
    return (x - m) / torch.sqrt(v + eps)
def _scatter_add(tgt, idx, src, axis=0):
    return tgt.clone().index_add_(axis, idx, src)
def _segment_sum(src, seg, n):
    out = torch.zeros((n,) + src.shape[1:], dtype=src.dtype)
    return out.index_add_(0, seg, src)
def _pad(x, pw):
    flat = tuple(v for pair in reversed(pw) for v in pair)
    return torch.nn.functional.pad(x, flat)

kernel_name = 'concat_heads__mha_block_cached__mha_block_cached__near_ridge'

def setup_inputs():

    _w = (lambda a: (lambda t: t.to(torch.bfloat16) if t.is_floating_point() else t)(torch.as_tensor(a)))
    def _A_setup():
        B, Sq, D = 8, 112, 1024
        H, Sk, Dh = 32, 512, 128
        return {
            "x": _w((lambda *s: torch.randn(*s, dtype=torch.bfloat16) * 0.8)(B, Sq, D)),
            "k_cache": _w((lambda *s: torch.randn(*s, dtype=torch.bfloat16) * 0.8)(B, H, Sk, Dh)),
            "v_cache": _w((lambda *s: torch.randn(*s, dtype=torch.bfloat16) * 0.05)(B, H, Sk, Dh)),
            "wq": _w((lambda *s: torch.randn(*s, dtype=torch.bfloat16) * 0.8)(D, H * Dh) * (D ** -0.5)),
            "wo": _w((lambda *s: torch.randn(*s, dtype=torch.bfloat16) * 0.05)(H * Dh, D) * ((H * Dh) ** -0.5)),
        }
    _A_in = _A_setup()

    def _B_setup():
        B, Sq, D = 8, 112, 1024
        H, Sk, Dh = 32, 512, 128
        return {
            "x": _w((lambda *s: torch.randn(*s, dtype=torch.bfloat16) * 0.8)(B, Sq, D)),
            "k_cache": _w((lambda *s: torch.randn(*s, dtype=torch.bfloat16) * 0.8)(B, H, Sk, Dh)),
            "v_cache": _w((lambda *s: torch.randn(*s, dtype=torch.bfloat16) * 0.05)(B, H, Sk, Dh)),
            "wq": _w((lambda *s: torch.randn(*s, dtype=torch.bfloat16) * 0.8)(D, H * Dh) * (D ** -0.5)),
            "wo": _w((lambda *s: torch.randn(*s, dtype=torch.bfloat16) * 0.05)(H * Dh, D) * ((H * Dh) ** -0.5)),
        }
    _B_in = _B_setup()

    return {
        "A_x": _A_in["x"],
        "A_k_cache": _A_in["k_cache"],
        "A_v_cache": _A_in["v_cache"],
        "A_wq": _A_in["wq"],
        "A_wo": _A_in["wo"],
        "B_x": _B_in["x"],
        "B_k_cache": _B_in["k_cache"],
        "B_v_cache": _B_in["v_cache"],
        "B_wq": _B_in["wq"],
        "B_wo": _B_in["wo"],
    }

def reference(A_x, A_k_cache, A_v_cache, A_wq, A_wo, B_x, B_k_cache, B_v_cache, B_wq, B_wo):
    def _A_ref(x, k_cache, v_cache, wq, wo):
        B, Sq, D = x.shape
        H, Dh = 32, 128
        q = (lambda x, *d: x.permute(*d))((x @ wq).reshape(B, Sq, H, Dh), 0, 2, 1, 3)
        scores = torch.einsum("bhqd,bhkd->bhqk", q, k_cache) * (Dh ** -0.5)
        p = _softmax(scores, axis=-1)
        o = torch.einsum("bhqk,bhkd->bhqd", p, v_cache)
        return (lambda x, *d: x.permute(*d))(o, 0, 2, 1, 3).reshape(B, Sq, H * Dh) @ wo

    def _B_ref(x, k_cache, v_cache, wq, wo):
        B, Sq, D = x.shape
        H, Dh = 32, 128
        q = (lambda x, *d: x.permute(*d))((x @ wq).reshape(B, Sq, H, Dh), 0, 2, 1, 3)
        scores = torch.einsum("bhqd,bhkd->bhqk", q, k_cache) * (Dh ** -0.5)
        p = _softmax(scores, axis=-1)
        o = torch.einsum("bhqk,bhkd->bhqd", p, v_cache)
        return (lambda x, *d: x.permute(*d))(o, 0, 2, 1, 3).reshape(B, Sq, H * Dh) @ wo

    a = _A_ref(A_x, A_k_cache, A_v_cache, A_wq, A_wo)
    b = _B_ref(B_x, B_k_cache, B_v_cache, B_wq, B_wo)
    return torch.concatenate([a, b], axis=-1)
```

```cpp
#include <hip/hip_runtime.h>
#include <hip/hip_bf16.h>
#include <stdint.h>

typedef __bf16 bf16x2 __attribute__((ext_vector_type(2)));
typedef __bf16 bf16x4 __attribute__((ext_vector_type(4)));
typedef __bf16 bf16x8 __attribute__((ext_vector_type(8)));
typedef float f32x2 __attribute__((ext_vector_type(2)));
typedef float f32x4 __attribute__((ext_vector_type(4)));
typedef unsigned u32x4 __attribute__((ext_vector_type(4)));
#define LAS __attribute__((address_space(3)))
#define DI __device__ __forceinline__

constexpr int NB = 8, SQ = 112, DM = 1024, NH = 32, SK = 512, DH = 128, HD = NH * DH;

#define K1_ROT 1
#define K1_PF0 2
#define K1_DEPTH 3

DI unsigned rfl(unsigned x) { return __builtin_amdgcn_readfirstlane(x); }
DI void dma16(__amdgpu_buffer_rsrc_t rsrc, unsigned voff, unsigned soff, unsigned lds) {
  asm volatile("s_mov_b32 m0, %0\n\ts_nop 0\n\tbuffer_load_dwordx4 %1, %2, %3 offen lds"
               :: "s"(lds), "v"(voff), "s"(rsrc), "s"(soff) : "m0", "memory");
}
DI void dma16nt(__amdgpu_buffer_rsrc_t rsrc, unsigned voff, unsigned soff, unsigned lds) {
  asm volatile("s_mov_b32 m0, %0\n\ts_nop 0\n\tbuffer_load_dwordx4 %1, %2, %3 offen nt lds"
               :: "s"(lds), "v"(voff), "s"(rsrc), "s"(soff) : "m0", "memory");
}
#define WAIT_VM(n) asm volatile("s_waitcnt vmcnt(" #n ")" ::: "memory")
#define BAR() do { asm volatile("" ::: "memory"); __builtin_amdgcn_s_waitcnt(0xC07F); asm volatile("s_barrier" ::: "memory"); } while (0)
#define SBAR() __builtin_amdgcn_sched_barrier(0)
DI bf16x8 lds_b128(unsigned a) { return *reinterpret_cast<LAS bf16x8*>(a); }
DI bf16x4 lds_tr(unsigned a) { return __builtin_amdgcn_ds_read_tr16_b64_v4bf16(reinterpret_cast<LAS bf16x4*>(a)); }
DI bf16x8 cat8(bf16x4 a, bf16x4 b) { return __builtin_shufflevector(a, b, 0, 1, 2, 3, 4, 5, 6, 7); }
DI unsigned cvtpk(float lo, float hi) {
  f32x2 v = {lo, hi};
  bf16x2 b = __builtin_convertvector(v, bf16x2);
  return __builtin_bit_cast(unsigned, b);
}
DI f32x4 mfma16(bf16x8 a, bf16x8 b, f32x4 c) { return __builtin_amdgcn_mfma_f32_16x16x32_bf16(a, b, c, 0, 0, 0); }
DI u32x4 pack_pair(f32x4 A, f32x4 B) {
  unsigned a0 = cvtpk(A[0], A[1]), a1 = cvtpk(A[2], A[3]), b0 = cvtpk(B[0], B[1]), b1 = cvtpk(B[2], B[3]);
  auto r0 = __builtin_amdgcn_permlane32_swap(a0, b0, false, false);
  auto s0 = __builtin_amdgcn_permlane16_swap(r0[0], r0[1], false, false);
  auto r1 = __builtin_amdgcn_permlane32_swap(a1, b1, false, false);
  auto s1 = __builtin_amdgcn_permlane16_swap(r1[0], r1[1], false, false);
  u32x4 o = {s0[0], s1[0], s0[1], s1[1]};
  return o;
}
DI float xmax_g(float v) {
  auto r = __builtin_amdgcn_permlane32_swap(__float_as_uint(v), __float_as_uint(v), false, false);
  v = fmaxf(__uint_as_float(r[0]), __uint_as_float(r[1]));
  auto s = __builtin_amdgcn_permlane16_swap(__float_as_uint(v), __float_as_uint(v), false, false);
  return fmaxf(__uint_as_float(s[0]), __uint_as_float(s[1]));
}
DI float xsum_g(float v) {
  auto r = __builtin_amdgcn_permlane32_swap(__float_as_uint(v), __float_as_uint(v), false, false);
  v = __uint_as_float(r[0]) + __uint_as_float(r[1]);
  auto s = __builtin_amdgcn_permlane16_swap(__float_as_uint(v), __float_as_uint(v), false, false);
  return __uint_as_float(s[0]) + __uint_as_float(s[1]);
}

constexpr unsigned SLOT = 16384;
constexpr unsigned L_WQ = 5 * SLOT;
constexpr unsigned L_X = 8 * SLOT;
constexpr unsigned XSZ = 14336;
constexpr unsigned L_FLAG = L_X + 2 * XSZ;
constexpr unsigned K1_LDS = 163840;
constexpr int NSLOT = 9;

struct QFragH { bf16x8 a[8]; bf16x8 b[2]; };
struct QFragL { bf16x8 a[16]; bf16x8 b[1]; };

template <int NT, int QT>
DI void q_read(bf16x8 (&a)[NT], bf16x8 (&b)[QT], unsigned wE, unsigned wO, unsigned xa) {
#pragma unroll
  for (int u = 0; u < QT; ++u) b[u] = lds_b128(xa + u * 2048);
#pragma unroll
  for (int t = 0; t < NT; ++t) {
    const unsigned base = (t & 1) ? wO : wE;
    bf16x4 lo = lds_tr(base + (t >> 1) * 256);
    bf16x4 hi = lds_tr(base + (t >> 1) * 256 + 2048);
    a[t] = cat8(lo, hi);
  }
}
template <int NT, int QT>
DI void q_mma(f32x4 (&acc)[NT][QT], const bf16x8 (&a)[NT], const bf16x8 (&b)[QT]) {
#pragma unroll
  for (int t = 0; t < NT; ++t)
#pragma unroll
    for (int u = 0; u < QT; ++u) acc[t][u] = mfma16(a[t], b[u], acc[t][u]);
}

template <int NT, int QT>
DI void q_sched() {
#pragma unroll
  for (int k = 0; k < 16; ++k) {
    __builtin_amdgcn_sched_group_barrier(0x008, 1, 0);
    __builtin_amdgcn_sched_group_barrier(0x100, (NT == 16) ? 2 : 1, 0);
  }
  __builtin_amdgcn_sched_group_barrier(0x100, 2, 0);
}
template <int NT, int QT>
DI void phase_q(f32x4 (&acc)[NT][QT], unsigned wE0, unsigned wO0, unsigned xa0,
                __amdgpu_buffer_rsrc_t rP, unsigned vp, unsigned pmask, int wave, unsigned lds0) {
  bf16x8 a0[NT], b0[QT], a1[NT], b1[QT];
#pragma unroll
  for (int t = 0; t < NT; ++t)
#pragma unroll
    for (int u = 0; u < QT; ++u) acc[t][u] = f32x4{0.f, 0.f, 0.f, 0.f};
  BAR();
  q_read<NT, QT>(a0, b0, wE0, wO0, xa0);
  unsigned ws = SLOT;
  for (int s = 0; s < 30; s += 2) {
    BAR();
    SBAR();
    q_read<NT, QT>(a1, b1, wE0 + ws, wO0 + ws, xa0 + ((s >> 1) & 1) * XSZ + 512);
    ws = (ws == 2 * SLOT) ? 0u : ws + SLOT;
    q_mma<NT, QT>(acc, a0, b0);
    q_sched<NT, QT>();
    SBAR();
    BAR();
    SBAR();
    q_read<NT, QT>(a0, b0, wE0 + ws, wO0 + ws, xa0 + (((s + 2) >> 1) & 1) * XSZ);
    ws = (ws == 2 * SLOT) ? 0u : ws + SLOT;
    q_mma<NT, QT>(acc, a1, b1);
    q_sched<NT, QT>();
    SBAR();
    if (NT == 8) {
      const int it = (s >> 1) - K1_PF0;
      if (wave < 5 && it >= 0 && it < 8) {
        const unsigned so = (unsigned)(wave >> 1) * 16384u + (unsigned)it * 2048u;
        const unsigned ld = lds0 + (unsigned)wave * SLOT + (unsigned)it * 2048u;
        const unsigned vo = vp ^ ((it & 1) ? pmask : 0u);
        dma16nt(rP, vo, so, ld);
        dma16nt(rP, vo, so + 128u, ld + 1024u);
      }
    }
  }
  BAR();
  SBAR();
  q_read<NT, QT>(a1, b1, wE0 + ws, wO0 + ws, xa0 + XSZ + 512);
  q_mma<NT, QT>(acc, a0, b0);
  q_sched<NT, QT>();
  SBAR();
  BAR();
  q_mma<NT, QT>(acc, a1, b1);
}

__global__ __launch_bounds__(512, 2) void k1_qattn(const __hip_bfloat16* __restrict__ xA, const __hip_bfloat16* __restrict__ kA,
                                                   const __hip_bfloat16* __restrict__ vA, const __hip_bfloat16* __restrict__ wqA,
                                                   const __hip_bfloat16* __restrict__ xB, const __hip_bfloat16* __restrict__ kB,
                                                   const __hip_bfloat16* __restrict__ vB, const __hip_bfloat16* __restrict__ wqB,
                                                   __hip_bfloat16* __restrict__ ows) {
  extern __shared__ __attribute__((aligned(16))) char shm[];
  const unsigned lds0 = (unsigned)(uintptr_t)(LAS char*)shm;
  const int tid = threadIdx.x, lane = tid & 63;
  const int wave = (int)rfl((unsigned)tid >> 6);
  const int bid = blockIdx.x, xcd = bid & 7, jj = bid >> 3;
  const int side = xcd >> 2, b = jj & 7, hpl = jj >> 3, hp = (xcd & 3) * 4 + hpl;
  const char* xg = (const char*)(side ? xB : xA) + (size_t)b * SQ * DM * 2;
  const char* wg = (const char*)(side ? wqB : wqA) + (size_t)hp * 512;
  const char* kg = (const char*)(side ? kB : kA) + (size_t)(b * NH + 2 * hp) * SK * DH * 2;
  const char* vg = (const char*)(side ? vB : vA) + (size_t)(b * NH + 2 * hp) * SK * DH * 2;
  const __amdgpu_buffer_rsrc_t rK = __builtin_amdgcn_make_buffer_rsrc((void*)kg, 0, 2 * SK * DH * 2, 0x00020000);
  const __amdgpu_buffer_rsrc_t rV = __builtin_amdgcn_make_buffer_rsrc((void*)vg, 0, 2 * SK * DH * 2, 0x00020000);

  const int i = lane & 15, g = lane >> 4, qq = (lane >> 2) & 3, p = lane & 3;
  const unsigned r8 = (lane >> 2) & 7;
  const unsigned vk0 = r8 * 256 + (lane >> 5) * 64 + ((lane & 3) ^ 0) * 16;
  const unsigned vk1 = r8 * 256 + (lane >> 5) * 64 + ((lane & 3) ^ 2) * 16;
  const unsigned vv = r8 * 256 + (lane >> 5) * 64 + ((lane & 3) ^ ((r8 >> 2) << 1)) * 16;

  volatile LAS unsigned* FULL = reinterpret_cast<volatile LAS unsigned*>(lds0 + L_FLAG);
  volatile LAS unsigned* PROG = reinterpret_cast<volatile LAS unsigned*>(lds0 + L_FLAG + 64);

  if (wave == 7) {
    __builtin_amdgcn_s_setprio(3);
    if (lane < 32) reinterpret_cast<volatile LAS unsigned*>(lds0 + L_FLAG)[lane] = 0u;
    const __amdgpu_buffer_rsrc_t rX = __builtin_amdgcn_make_buffer_rsrc((void*)xg, 0, SQ * DM * 2, 0x00020000);
    const __amdgpu_buffer_rsrc_t rW = __builtin_amdgcn_make_buffer_rsrc((void*)wg, 0, (unsigned)((size_t)DM * HD * 2 - (size_t)hp * 512), 0x00020000);
    const unsigned vw0 = qq * 8192 + g * 64 + (p ^ 0) * 16;
    const unsigned vw1 = qq * 8192 + g * 64 + (p ^ 2) * 16;
    const unsigned vx0 = r8 * 2048 + (lane >> 5) * 64 + (p ^ 0) * 16;
    const unsigned vx1 = r8 * 2048 + (lane >> 5) * 64 + (p ^ 2) * 16;
    const int rot = K1_ROT ? ((4 * b + 8 * hpl) & 31) : 0;
    auto issue_wq = [&](int s, unsigned slot) {
      const unsigned ks = (unsigned)((s + rot) & 31);
      const unsigned so = ks * 32u * 8192u, ld = lds0 + L_WQ + slot;
#pragma unroll
      for (int pc = 0; pc < 16; ++pc)
        dma16(rW, ((pc >> 2) & 1) ? vw1 : vw0, so + (unsigned)(pc >> 1) * 4u * 8192u + (unsigned)(pc & 1) * 256u, ld + pc * 1024u);
    };
    auto issue_x = [&](int xs) {
      const unsigned kx = (unsigned)((xs + (rot >> 1)) & 15);
      const unsigned so = kx * 128u, ld = lds0 + L_X + (unsigned)(xs & 1) * XSZ;
#pragma unroll
      for (int R = 0; R < 14; ++R) dma16(rX, (R & 1) ? vx1 : vx0, so + (unsigned)R * 8u * 2048u, ld + R * 1024u);
    };
    auto issue_kv = [&](int n) {
      const unsigned so = (unsigned)(n >> 1) * 16384u, ld = lds0 + (unsigned)(n % NSLOT) * SLOT;
      if (n & 1) {
#pragma unroll
        for (int pc = 0; pc < 16; ++pc) dma16nt(rV, vv, so + (unsigned)(pc >> 1) * 2048u + (unsigned)(pc & 1) * 128u, ld + pc * 1024u);
      } else {
#pragma unroll
        for (int pc = 0; pc < 16; ++pc)
          dma16nt(rK, (pc & 2) ? vk1 : vk0, so + (unsigned)(pc >> 1) * 2048u + (unsigned)(pc & 1) * 128u, ld + pc * 1024u);
      }
    };
    issue_x(0); issue_wq(0, 0); issue_wq(1, SLOT); issue_x(1); issue_wq(2, 2 * SLOT);
    WAIT_VM(46);
    BAR();
    unsigned ws = 0;
    for (int s = 0; s < 30; s += 2) {
      WAIT_VM(30);
      BAR();
      issue_wq(s + 3, ws);
      ws = (ws == 2 * SLOT) ? 0u : ws + SLOT;
      WAIT_VM(16);
      BAR();
      if (s + 4 <= 31) issue_wq(s + 4, ws);
      ws = (ws == 2 * SLOT) ? 0u : ws + SLOT;
      if ((s + 4) / 2 <= 15) issue_x((s + 4) / 2);
    }
    WAIT_VM(0);
    BAR();
    BAR();
    issue_kv(5); issue_kv(6); issue_kv(7);
    BAR();
    for (int n = 8; n < 32; ++n) {
      WAIT_VM(32);
      if (lane == 0) *FULL = (unsigned)(n - 2);
      const unsigned need = (n - 8 > 1) ? (unsigned)(n - 8) : 1u;
      for (int it = 0; it < (1 << 22); ++it) {
        const unsigned v = (lane < 7) ? PROG[lane] : 0xffffffffu;
        if (__all(v >= need)) break;
        __builtin_amdgcn_s_sleep(1);
      }
      asm volatile("" ::: "memory");
      issue_kv(n);
    }
    WAIT_VM(32);
    if (lane == 0) *FULL = 30u;
    WAIT_VM(16);
    if (lane == 0) *FULL = 31u;
    WAIT_VM(0);
    if (lane == 0) *FULL = 32u;
    return;
  }

  bf16x8 qf[2][4];
  int u;
  {
    const unsigned wb = lds0 + L_WQ + g * 4096 + qq * 64 + p * 8;
    const unsigned xb = lds0 + L_X + (i >> 3) * 1024 + (i & 7) * 64 + ((g ^ ((i >> 3) << 1)) * 16);
    const unsigned xch = lds0 + L_X;
    if (wave < 6) {
      const int hh = wave / 3, w3 = wave - 3 * hh;
      f32x4 acc[8][2];
      const unsigned wE = wb + hh * 1024 + ((g & 1) * 32), wO = wb + hh * 1024 + (((g & 1) ^ 1) * 32);
      const __amdgpu_buffer_rsrc_t rP = __builtin_amdgcn_make_buffer_rsrc((void*)((wave & 1) ? vg : kg), 0, 2 * SK * DH * 2, 0x00020000);
      phase_q<8, 2>(acc, wE, wO, xb + w3 * 4096, rP, (wave & 1) ? vv : vk0, (wave & 1) ? 0u : 32u, wave, lds0);
      u = 2 * w3 + hh;
      u32x4 keep[4], give[4];
#pragma unroll
      for (int s = 0; s < 4; ++s) {
        u32x4 t0 = pack_pair(acc[2 * s][0], acc[2 * s + 1][0]);
        u32x4 t1 = pack_pair(acc[2 * s][1], acc[2 * s + 1][1]);
        keep[s] = hh ? t1 : t0;
        give[s] = hh ? t0 : t1;
      }
#pragma unroll
      for (int s = 0; s < 4; ++s) *reinterpret_cast<LAS u32x4*>(xch + wave * 4096 + s * 1024 + lane * 16) = give[s];
      WAIT_VM(0);
      BAR();
      const int partner = hh ? wave - 3 : wave + 3;
#pragma unroll
      for (int s = 0; s < 4; ++s) {
        u32x4 o = *reinterpret_cast<LAS u32x4*>(xch + partner * 4096 + s * 1024 + lane * 16);
        u32x4 h0 = hh ? o : keep[s], h1 = hh ? keep[s] : o;
        qf[0][s] = __builtin_bit_cast(bf16x8, h0);
        qf[1][s] = __builtin_bit_cast(bf16x8, h1);
      }
    } else {
      f32x4 acc[16][1];
      const unsigned wE = wb + ((g & 1) * 32), wO = wb + (((g & 1) ^ 1) * 32);
      phase_q<16, 1>(acc, wE, wO, xb + 6 * 2048, rK, 0u, 0u, wave, lds0);
      u = 6;
#pragma unroll
      for (int s = 0; s < 4; ++s) {
        qf[0][s] = __builtin_bit_cast(bf16x8, pack_pair(acc[2 * s][0], acc[2 * s + 1][0]));
        qf[1][s] = __builtin_bit_cast(bf16x8, pack_pair(acc[8 + 2 * s][0], acc[8 + 2 * s + 1][0]));
      }
      WAIT_VM(0);
      BAR();
    }
  }

  const unsigned kb = lds0 + (i >> 3) * 2048 + (i & 7) * 64 + ((g ^ ((i >> 3) << 1)) * 16);
  const unsigned vb0 = lds0 + (g >> 1) * 2048 + (4 * (g & 1) + qq) * 64 + p * 8;
  const unsigned vbE = vb0 + (g & 1) * 32, vbO = vb0 + ((g & 1) ^ 1) * 32;
  constexpr float SC = 0.08838834764831845f * 1.4426950408889634f;
  auto wait_tile = [&](int j) {
    if (j >= 5) {
      for (int it = 0; it < (1 << 22); ++it) {
        const unsigned v = *FULL;
        if (rfl(v) > (unsigned)j) break;
        __builtin_amdgcn_s_sleep(1);
      }
    }
    asm volatile("" ::: "memory");
  };
  unsigned slot = 0;
#pragma unroll
  for (int hh = 0; hh < 2; ++hh) {
    f32x4 o[8];
#pragma unroll
    for (int d = 0; d < 8; ++d) o[d] = f32x4{0.f, 0.f, 0.f, 0.f};
    float m = -__builtin_inff(), l = 0.f;
    for (int t = 0; t < 8; ++t) {
      const int j = hh * 16 + 2 * t;
      wait_tile(j);
      f32x4 s[4];
#pragma unroll
      for (int kt = 0; kt < 4; ++kt) {
        s[kt] = f32x4{0.f, 0.f, 0.f, 0.f};
#pragma unroll
        for (int ks = 0; ks < 4; ++ks)
          s[kt] = mfma16(lds_b128(kb + slot + kt * 4096 + (ks >> 1) * 1024 + (ks & 1) * 512), qf[hh][ks], s[kt]);
      }
      asm volatile("" ::: "memory");
      if (lane == 0) PROG[wave] = (unsigned)(j + 1);
      slot = (slot == (NSLOT - 1) * SLOT) ? 0u : slot + SLOT;
      float mx = fmaxf(fmaxf(s[0][0], s[0][1]), fmaxf(s[0][2], s[0][3]));
#pragma unroll
      for (int kt = 1; kt < 4; ++kt) mx = fmaxf(mx, fmaxf(fmaxf(s[kt][0], s[kt][1]), fmaxf(s[kt][2], s[kt][3])));
      mx = xmax_g(mx);
      const float mn = fmaxf(m, mx);
      const float alpha = __builtin_amdgcn_exp2f((m - mn) * SC);
      m = mn;
      const float nb = -mn * SC;
      float ps = 0.f;
#pragma unroll
      for (int kt = 0; kt < 4; ++kt)
#pragma unroll
        for (int r = 0; r < 4; ++r) {
          s[kt][r] = __builtin_amdgcn_exp2f(fmaf(s[kt][r], SC, nb));
          ps += s[kt][r];
        }
      l = l * alpha + ps;
#pragma unroll
      for (int d = 0; d < 8; ++d) o[d] *= alpha;
      bf16x8 pf[2];
#pragma unroll
      for (int ks = 0; ks < 2; ++ks) {
        u32x4 w = {cvtpk(s[2 * ks][0], s[2 * ks][1]), cvtpk(s[2 * ks][2], s[2 * ks][3]),
                   cvtpk(s[2 * ks + 1][0], s[2 * ks + 1][1]), cvtpk(s[2 * ks + 1][2], s[2 * ks + 1][3])};
        pf[ks] = __builtin_bit_cast(bf16x8, w);
      }
      wait_tile(j + 1);
#pragma unroll
      for (int d = 0; d < 8; ++d) {
        const unsigned base = ((d & 1) ? vbO : vbE) + slot + (d >> 1) * 512;
#pragma unroll
        for (int ks = 0; ks < 2; ++ks) {
          bf16x4 lo = lds_tr(base + (4 * ks) * 2048), hi = lds_tr(base + (4 * ks + 2) * 2048);
          o[d] = mfma16(cat8(lo, hi), pf[ks], o[d]);
        }
      }
      asm volatile("" ::: "memory");
      if (lane == 0) PROG[wave] = (unsigned)(j + 2);
      slot = (slot == (NSLOT - 1) * SLOT) ? 0u : slot + SLOT;
    }
    const float inv = 1.0f / xsum_g(l);
    char* op = (char*)ows + ((size_t)(side * NB * SQ + b * SQ + 16 * u + i) * HD + (2 * hp + hh) * DH + 8 * g) * 2;
#pragma unroll
    for (int s = 0; s < 4; ++s) {
      u32x4 w = pack_pair(o[2 * s] * inv, o[2 * s + 1] * inv);
      *reinterpret_cast<u32x4*>(op + s * 64) = w;
    }
  }
}

constexpr unsigned ST_O = 14336, ST_W = 8192, STG = ST_O + ST_W;
constexpr int NS2 = 7;
constexpr unsigned K2_LDS = NS2 * STG;

template <int MT>
DI void k2_body(f32x4 (&acc)[2][4], unsigned wE, unsigned wO, unsigned ob) {
  bf16x8 a[2], bb[MT];
#pragma unroll
  for (int u = 0; u < MT; ++u) bb[u] = lds_b128(ob + u * 2048);
  a[0] = cat8(lds_tr(wE), lds_tr(wE + 512));
  a[1] = cat8(lds_tr(wO), lds_tr(wO + 512));
#pragma unroll
  for (int u = 0; u < MT; ++u) {
    acc[0][u] = mfma16(a[0], bb[u], acc[0][u]);
    acc[1][u] = mfma16(a[1], bb[u], acc[1][u]);
  }
}

__global__ __launch_bounds__(512, 2) void k2_oproj(const __hip_bfloat16* __restrict__ ows, const __hip_bfloat16* __restrict__ woA,
                                                   const __hip_bfloat16* __restrict__ woB, __hip_bfloat16* __restrict__ out) {
  extern __shared__ __attribute__((aligned(16))) char shm[];
  const unsigned lds0 = (unsigned)(uintptr_t)(LAS char*)shm;
  const int tid = threadIdx.x, lane = tid & 63;
  const int wave = (int)rfl((unsigned)tid >> 6);
  const int bid = blockIdx.x, xcd = bid & 7, jj = bid >> 3;
  const int side = xcd >> 2, b = ((xcd >> 1) & 1) * 4 + (jj & 3), nt = (xcd & 1) * 8 + (jj >> 2);
  const char* og = (const char*)ows + (size_t)(side * NB + b) * SQ * HD * 2;
  const char* wg = (const char*)(side ? woB : woA) + (size_t)nt * 128;
  const __amdgpu_buffer_rsrc_t rO = __builtin_amdgcn_make_buffer_rsrc((void*)og, 0, SQ * HD * 2, 0x00020000);
  const __amdgpu_buffer_rsrc_t rW = __builtin_amdgcn_make_buffer_rsrc((void*)wg, 0, (unsigned)(HD * DM * 2 - nt * 128), 0x00020000);
  const int i = lane & 15, g = lane >> 4, qq = (lane >> 2) & 3, p = lane & 3;
  const unsigned r8 = (lane >> 2) & 7;
  const unsigned vo0 = r8 * 8192 + (lane >> 5) * 64 + (p ^ 0) * 16;
  const unsigned vo1 = r8 * 8192 + (lane >> 5) * 64 + (p ^ 2) * 16;
  const unsigned vw = (4 * (lane >> 5) + qq) * 2048 + ((lane >> 4) & 1) * 64 + (p ^ ((wave & 1) << 1)) * 16;
  auto issue = [&](int s, unsigned slot) {
    const unsigned oob = (s < 64) ? 0u : 0x40000000u;
    const unsigned ld = lds0 + slot;
    if (wave < 7) {
      dma16(rO, vo0, oob + (unsigned)s * 128u + (unsigned)(2 * wave) * 8u * 8192u, ld + (unsigned)(2 * wave) * 1024u);
      dma16(rO, vo1, oob + (unsigned)s * 128u + (unsigned)(2 * wave + 1) * 8u * 8192u, ld + (unsigned)(2 * wave + 1) * 1024u);
    }
    dma16(rW, vw, oob + ((unsigned)s * 64u + (unsigned)wave * 8u) * 2048u, ld + ST_O + (unsigned)wave * 1024u);
  };
  const int kh = wave >> 2, nh = wave & 1, mg = ((wave >> 1) & 1) ^ kh;
  const unsigned wb = lds0 + ST_O + kh * 4096 + nh * 256 + g * 1024 + qq * 64 + p * 8;
  const unsigned wE = wb + (g & 1) * 32, wO = wb + ((g & 1) ^ 1) * 32;
  const unsigned ob = lds0 + mg * 8192 + kh * 512 + (i >> 3) * 1024 + (i & 7) * 64 + ((g ^ ((i >> 3) << 1)) * 16);
  f32x4 acc[2][4];
#pragma unroll
  for (int n = 0; n < 2; ++n)
#pragma unroll
    for (int u = 0; u < 4; ++u) acc[n][u] = f32x4{0.f, 0.f, 0.f, 0.f};
#pragma unroll
  for (int s = 0; s < NS2 - 1; ++s) issue(s, s * STG);
  unsigned cur = 0, nxt = (NS2 - 1) * STG;
  for (int s = 0; s < 64; ++s) {
    if (wave < 7) WAIT_VM(15); else WAIT_VM(5);
    BAR();
    issue(s + NS2 - 1, nxt);
    if (mg == 0) k2_body<4>(acc, wE + cur, wO + cur, ob + cur);
    else k2_body<3>(acc, wE + cur, wO + cur, ob + cur);
    nxt = cur;
    cur = (cur == (NS2 - 1) * STG) ? 0u : cur + STG;
  }
  WAIT_VM(0);
  BAR();
  const unsigned red = lds0 + (unsigned)((mg * 2 + nh) * 8192) + lane * 16;
  if (kh == 1) {
#pragma unroll
    for (int n = 0; n < 2; ++n)
#pragma unroll
      for (int u = 0; u < 4; ++u) *reinterpret_cast<LAS f32x4*>(red + (n * 4 + u) * 1024) = acc[n][u];
  }
  BAR();
  if (kh == 0) {
    const int MT = mg ? 3 : 4;
#pragma unroll
    for (int u = 0; u < 4; ++u) {
      if (u < MT) {
        f32x4 c0 = acc[0][u] + *reinterpret_cast<LAS f32x4*>(red + (0 * 4 + u) * 1024);
        f32x4 c1 = acc[1][u] + *reinterpret_cast<LAS f32x4*>(red + (1 * 4 + u) * 1024);
        u32x4 w = pack_pair(c0, c1);
        char* op = (char*)out + ((size_t)(b * SQ + mg * 64 + u * 16 + i) * (2 * DM) + side * DM + nt * 64 + nh * 32 + 8 * g) * 2;
        *reinterpret_cast<u32x4*>(op) = w;
      }
    }
  }
}

extern "C" void kernel_launch(void* const* d_in, const int* in_sizes, int n_in, void* d_out, int out_size, void* d_ws, size_t ws_size,
                              hipStream_t stream) {
  (void)in_sizes; (void)n_in; (void)out_size; (void)ws_size;
  typedef const __hip_bfloat16* cp;
  (void)hipFuncSetAttribute((const void*)k1_qattn, hipFuncAttributeMaxDynamicSharedMemorySize, (int)K1_LDS);
  (void)hipFuncSetAttribute((const void*)k2_oproj, hipFuncAttributeMaxDynamicSharedMemorySize, (int)K2_LDS);
  __hip_bfloat16* ows = (__hip_bfloat16*)d_ws;
  k1_qattn<<<256, 512, K1_LDS, stream>>>((cp)d_in[0], (cp)d_in[1], (cp)d_in[2], (cp)d_in[3], (cp)d_in[5], (cp)d_in[6], (cp)d_in[7],
                                         (cp)d_in[8], ows);
  k2_oproj<<<256, 512, K2_LDS, stream>>>(ows, (cp)d_in[4], (cp)d_in[9], (__hip_bfloat16*)d_out);
}
```

```cpp
#include <hip/hip_runtime.h>
#include <hip/hip_bf16.h>
#include <stdint.h>

typedef __bf16 bf16x2 __attribute__((ext_vector_type(2)));
typedef __bf16 bf16x4 __attribute__((ext_vector_type(4)));
typedef __bf16 bf16x8 __attribute__((ext_vector_type(8)));
typedef float f32x2 __attribute__((ext_vector_type(2)));
typedef float f32x4 __attribute__((ext_vector_type(4)));
typedef unsigned u32x4 __attribute__((ext_vector_type(4)));
#define LAS __attribute__((address_space(3)))
#define DI __device__ __forceinline__

constexpr size_t WS_PART = (size_t)16 << 20, WS_FLAG = (size_t)48 << 20;
constexpr int NB = 8, SQ = 112, DM = 1024, NH = 32, SK = 512, DH = 128, HD = NH * DH;

#define K1_ROT 1
#define K1_PF0 2
#define K1_DEPTH 3

DI unsigned rfl(unsigned x) { return __builtin_amdgcn_readfirstlane(x); }
DI void dma16(__amdgpu_buffer_rsrc_t rsrc, unsigned voff, unsigned soff, unsigned lds) {
  asm volatile("s_mov_b32 m0, %0\n\ts_nop 0\n\tbuffer_load_dwordx4 %1, %2, %3 offen lds"
               :: "s"(lds), "v"(voff), "s"(rsrc), "s"(soff) : "m0", "memory");
}
DI void dma16nt(__amdgpu_buffer_rsrc_t rsrc, unsigned voff, unsigned soff, unsigned lds) {
  asm volatile("s_mov_b32 m0, %0\n\ts_nop 0\n\tbuffer_load_dwordx4 %1, %2, %3 offen nt lds"
               :: "s"(lds), "v"(voff), "s"(rsrc), "s"(soff) : "m0", "memory");
}
#define K1_DNT 1
#define K1_DYNP 2
#define K1_WIN 4
#define DPOL " nt"
DI void dma16d(__amdgpu_buffer_rsrc_t rsrc, unsigned voff, unsigned soff, unsigned lds) {
  asm volatile("s_mov_b32 m0, %0\n\ts_nop 0\n\tbuffer_load_dwordx4 %1, %2, %3 offen" DPOL " lds"
               :: "s"(lds), "v"(voff), "s"(rsrc), "s"(soff) : "m0", "memory");
}
DI void dma16x4(__amdgpu_buffer_rsrc_t rsrc, unsigned v0, unsigned v1, unsigned v2, unsigned v3, unsigned soff, unsigned lds) {
  asm volatile("s_mov_b32 m0, %0\n\ts_nop 0\n\t"
               "buffer_load_dwordx4 %1, %5, %6 offen" DPOL " lds\n\t"
               "buffer_load_dwordx4 %2, %5, %6 offen offset:1024" DPOL " lds\n\t"
               "buffer_load_dwordx4 %3, %5, %6 offen offset:2048" DPOL " lds\n\t"
               "buffer_load_dwordx4 %4, %5, %6 offen offset:3072" DPOL " lds"
               :: "s"(lds), "v"(v0), "v"(v1), "v"(v2), "v"(v3), "s"(rsrc), "s"(soff) : "m0", "memory");
}
DI void dma16x3(__amdgpu_buffer_rsrc_t rsrc, unsigned v0, unsigned v1, unsigned v2, unsigned soff, unsigned lds) {
  asm volatile("s_mov_b32 m0, %0\n\ts_nop 0\n\t"
               "buffer_load_dwordx4 %1, %4, %5 offen" DPOL " lds\n\t"
               "buffer_load_dwordx4 %2, %4, %5 offen offset:1024" DPOL " lds\n\t"
               "buffer_load_dwordx4 %3, %4, %5 offen offset:2048" DPOL " lds"
               :: "s"(lds), "v"(v0), "v"(v1), "v"(v2), "s"(rsrc), "s"(soff) : "m0", "memory");
}
__device__ const unsigned SEQ[64] = {0, 1, 2, 3, 4, 5, 6, 7, 8, 9, 10, 11, 12, 13, 14, 15, 16, 17, 18, 19, 20, 21, 22, 23, 24, 25, 26, 27, 28, 29, 30, 31,
                                     32, 33, 34, 35, 36, 37, 38, 39, 40, 41, 42, 43, 44, 45, 46, 47, 48, 49, 50, 51, 52, 53, 54, 55, 56, 57, 58, 59, 60, 61, 62, 63};
DI void dma_flag(__amdgpu_buffer_rsrc_t rseq, unsigned val, unsigned lds) {
  asm volatile("s_mov_b32 m0, %0\n\ts_mov_b64 exec, 1\n\tbuffer_load_dword off, %1, %2 lds\n\ts_mov_b64 exec, -1"
               :: "s"(lds), "s"(rseq), "s"(val * 4u) : "m0", "memory");
}
DI void touch4(__amdgpu_buffer_rsrc_t rsrc, unsigned voff, unsigned soff, unsigned lds) {
  asm volatile("s_mov_b32 m0, %0\n\ts_nop 0\n\tbuffer_load_dword %1, %2, %3 offen lds"
               :: "s"(lds), "v"(voff), "s"(rsrc), "s"(soff) : "m0", "memory");
}
DI void touch4nt(__amdgpu_buffer_rsrc_t rsrc, unsigned voff, unsigned soff, unsigned lds) {
  asm volatile("s_mov_b32 m0, %0\n\ts_nop 0\n\tbuffer_load_dword %1, %2, %3 offen nt lds"
               :: "s"(lds), "v"(voff), "s"(rsrc), "s"(soff) : "m0", "memory");
}
#define WAIT_VM(n) asm volatile("s_waitcnt vmcnt(" #n ")" ::: "memory")
#define BAR() do { asm volatile("" ::: "memory"); __builtin_amdgcn_s_waitcnt(0xC07F); asm volatile("s_barrier" ::: "memory"); } while (0)
#define SBAR() __builtin_amdgcn_sched_barrier(0)
DI void lds_pub(unsigned addr, unsigned val) {
  asm volatile("s_mov_b64 exec, 1\n\tds_write_b32 %0, %1\n\ts_mov_b64 exec, -1" :: "v"(addr), "v"(val) : "memory");
}
DI void lds_wait_ge(unsigned addr, unsigned need) {
  unsigned tmp, cnt = 1u << 22;
  asm volatile(
      "LW%=:\n\t"
      "ds_read_b32 %0, %2\n\t"
      "s_waitcnt lgkmcnt(0)\n\t"
      "v_cmp_le_u32 vcc, %3, %0\n\t"
      "s_sub_u32 %1, %1, 1\n\t"
      "s_cbranch_scc1 LE%=\n\t"
      "s_cmp_eq_u64 vcc, -1\n\t"
      "s_cbranch_scc1 LE%=\n\t"
      "s_sleep 1\n\t"
      "s_branch LW%=\n\t"
      "LE%=:"
      : "=&v"(tmp), "+s"(cnt) : "v"(addr), "s"(need) : "memory", "scc", "vcc");
}
DI bf16x8 lds_b128(unsigned a) { return *reinterpret_cast<LAS bf16x8*>(a); }
DI bf16x4 lds_tr(unsigned a) { return __builtin_amdgcn_ds_read_tr16_b64_v4bf16(reinterpret_cast<LAS bf16x4*>(a)); }
DI bf16x8 cat8(bf16x4 a, bf16x4 b) { return __builtin_shufflevector(a, b, 0, 1, 2, 3, 4, 5, 6, 7); }
DI unsigned cvtpk(float lo, float hi) {
  f32x2 v = {lo, hi};
  bf16x2 b = __builtin_convertvector(v, bf16x2);
  return __builtin_bit_cast(unsigned, b);
}
DI f32x4 mfma16(bf16x8 a, bf16x8 b, f32x4 c) { return __builtin_amdgcn_mfma_f32_16x16x32_bf16(a, b, c, 0, 0, 0); }
DI u32x4 pack_pair(f32x4 A, f32x4 B) {
  unsigned a0 = cvtpk(A[0], A[1]), a1 = cvtpk(A[2], A[3]), b0 = cvtpk(B[0], B[1]), b1 = cvtpk(B[2], B[3]);
  auto r0 = __builtin_amdgcn_permlane32_swap(a0, b0, false, false);
  auto s0 = __builtin_amdgcn_permlane16_swap(r0[0], r0[1], false, false);
  auto r1 = __builtin_amdgcn_permlane32_swap(a1, b1, false, false);
  auto s1 = __builtin_amdgcn_permlane16_swap(r1[0], r1[1], false, false);
  u32x4 o = {s0[0], s1[0], s0[1], s1[1]};
  return o;
}
DI float xmax_g(float v) {
  auto r = __builtin_amdgcn_permlane32_swap(__float_as_uint(v), __float_as_uint(v), false, false);
  v = fmaxf(__uint_as_float(r[0]), __uint_as_float(r[1]));
  auto s = __builtin_amdgcn_permlane16_swap(__float_as_uint(v), __float_as_uint(v), false, false);
  return fmaxf(__uint_as_float(s[0]), __uint_as_float(s[1]));
}
DI float xsum_g(float v) {
  auto r = __builtin_amdgcn_permlane32_swap(__float_as_uint(v), __float_as_uint(v), false, false);
  v = __uint_as_float(r[0]) + __uint_as_float(r[1]);
  auto s = __builtin_amdgcn_permlane16_swap(__float_as_uint(v), __float_as_uint(v), false, false);
  return __uint_as_float(s[0]) + __uint_as_float(s[1]);
}

#define STAMP(idx) do {} while (0)
#define STAMP2(idx) do {} while (0)
#define STAMPC(idx) do {} while (0)
#define TACC(acc, last) do {} while (0)
#define TPUT(idx, v) do {} while (0)
DI unsigned long long tnow() { return 0; }
constexpr unsigned SLOT = 16384;
constexpr unsigned DSZ = 47104;
constexpr unsigned L_FLAG = 9 * SLOT;
constexpr unsigned L_QX = L_FLAG + 1024;
constexpr unsigned K1_LDS = L_QX + 8192;
#define K1_TNT 1
#define K1_LPRIO 3
#define K1_JT1 0
#define K1_JT2 5
#define K1_JB0 3
#define K1_JB1 7
#define K1_JB2 11
#define K1_JBD 1

DI void gtouch(const char* p, unsigned lds) {
  if (K1_TNT) asm volatile("s_mov_b32 m0, %0\n\ts_nop 0\n\tglobal_load_lds_dword %1, off nt" :: "s"(lds), "v"(p) : "m0", "memory");
  else asm volatile("s_mov_b32 m0, %0\n\ts_nop 0\n\tglobal_load_lds_dword %1, off" :: "s"(lds), "v"(p) : "m0", "memory");
}
DI unsigned ring_slot(int hh, int r) { return (((hh ? 0x2874u : 0x10653u) >> (4 * r)) & 15u) * SLOT; }

template <int QT>
DI void q_read(bf16x8 (&a)[8], bf16x8 (&b)[QT], unsigned wE, unsigned wO, unsigned xa) {
  asm volatile("" : "+v"(wE), "+v"(wO), "+v"(xa));
#pragma unroll
  for (int u = 0; u < QT; ++u) b[u] = lds_b128(xa + u * 2048);
#pragma unroll
  for (int t = 0; t < 8; ++t) {
    const unsigned base = (t & 1) ? wO : wE;
    a[t] = cat8(lds_tr(base + (t >> 1) * 256), lds_tr(base + (t >> 1) * 256 + 2048));
  }
}
template <int QT>
DI void q_mma(f32x4 (&acc)[8][QT], const bf16x8 (&a)[8], const bf16x8 (&b)[QT]) {
#pragma unroll
  for (int t = 0; t < 8; ++t)
#pragma unroll
    for (int u = 0; u < QT; ++u) acc[t][u] = mfma16(a[t], b[u], acc[t][u]);
}
template <int QT>
DI void q_sched() {
#pragma unroll
  for (int k = 0; k < 10; ++k) {
    __builtin_amdgcn_sched_group_barrier(0x008, 1, 0);
    __builtin_amdgcn_sched_group_barrier(0x100, 2, 0);
  }
  __builtin_amdgcn_sched_group_barrier(0x008, 8 * QT - 10, 0);
}

constexpr float SM_SC = 0.08838834764831845f * 1.4426950408889634f;
struct K1Ctx {
  const char *xg, *wg, *kg, *vg;
  unsigned lds0;
  int wave, hh, w3, lane, b, hpl, hp, jj, bd, rot;
  char* ows;
};

DI void dma16x4nt(__amdgpu_buffer_rsrc_t rsrc, unsigned v0, unsigned v1, unsigned v2, unsigned v3, unsigned soff, unsigned lds) {
  asm volatile("s_mov_b32 m0, %0\n\ts_nop 0\n\t"
               "buffer_load_dwordx4 %1, %5, %6 offen nt lds\n\t"
               "buffer_load_dwordx4 %2, %5, %6 offen offset:1024 nt lds\n\t"
               "buffer_load_dwordx4 %3, %5, %6 offen offset:2048 nt lds\n\t"
               "buffer_load_dwordx4 %4, %5, %6 offen offset:3072 nt lds"
               :: "s"(lds), "v"(v0), "v"(v1), "v"(v2), "v"(v3), "s"(rsrc), "s"(soff) : "m0", "memory");
}
DI float max3f(float a, float b, float c) { return fmaxf(fmaxf(a, b), c); }
#define K1_OFF 16.0f

template <int QT, int NS>
DI void phase_a(const K1Ctx& c, const bf16x8 (&qf)[QT][4], char* op, unsigned vk0, unsigned vv) {
  const int lane = c.lane, w3 = c.w3, hh = c.hh;
  const unsigned lds0 = c.lds0;
  const int i = lane & 15, g = lane >> 4, qq = (lane >> 2) & 3, p = lane & 3;
  const unsigned aLAND = lds0 + L_FLAG + 16u * (unsigned)hh, aPROG = lds0 + L_FLAG + 32u + 16u * (unsigned)hh;
  const unsigned kb = lds0 + (i >> 3) * 2048 + (i & 7) * 64 + ((g ^ ((i >> 3) << 1)) * 16);
  const unsigned vb0 = lds0 + (g >> 1) * 2048 + (4 * (g & 1) + qq) * 64 + p * 8;
  const unsigned vbE = vb0 + (g & 1) * 32, vbO = vb0 + ((g & 1) ^ 1) * 32;
  const unsigned gp = aPROG + 4u * (unsigned)(lane & 3), myp = aPROG + 4u * (unsigned)w3;
  const __amdgpu_buffer_rsrc_t rK = __builtin_amdgcn_make_buffer_rsrc((void*)(c.kg - 1024), 0, SK * DH * 2 + 1024, 0x00020000);
  const __amdgpu_buffer_rsrc_t rV = __builtin_amdgcn_make_buffer_rsrc((void*)(c.vg - 1024), 0, SK * DH * 2 + 1024, 0x00020000);
  const __amdgpu_buffer_rsrc_t rS = __builtin_amdgcn_make_buffer_rsrc((void*)SEQ, 0, 256, 0x00020000);
  const unsigned k0 = vk0 + 1024u, k1 = vk0 + 128u, k2 = (vk0 ^ 32u) + 1024u, k3 = (vk0 ^ 32u) + 128u;
  const unsigned v0 = vv + 1024u, v1 = vv + 128u;
  auto issue = [&](int m, unsigned sl) {
    const unsigned so = (unsigned)(m >> 1) * 16384u;
    const unsigned ld = lds0 + sl;
    if (m & 1) {
#pragma unroll
      for (int k = 0; k < 4; ++k) dma16x4nt(rV, v0, v1, v0, v1, so + k * 4096u, ld + k * 4096u);
    } else {
#pragma unroll
      for (int k = 0; k < 4; ++k) dma16x4nt(rK, k0, k1, k2, k3, so + k * 4096u, ld + k * 4096u);
    }
    dma_flag(rS, (unsigned)(m + 1), aLAND);
  };
  int rp = 0;
  unsigned slot = ring_slot(hh, 0);
  auto adv = [&]() { rp = (rp == NS - 1) ? 0 : rp + 1; slot = ring_slot(hh, rp); };
  int rq = 0;
  auto refill = [&](int m) {
    if (QT == 1) {
      if (m + NS < 16) {
        lds_wait_ge(gp, (unsigned)(m + 1));
        issue(m + NS, ring_slot(hh, rq));
      }
      rq = (rq == NS - 1) ? 0 : rq + 1;
    }
  };
  if (QT == 1) {
    lds_pub(aLAND, 3u);
    issue(3, ring_slot(hh, 3));
    if (NS == 5) issue(4, ring_slot(hh, 4));
  }
  bf16x8 kf[16], vf[16];
  auto readK = [&](int half) {
    unsigned ka = kb + slot;
    asm volatile("" : "+v"(ka));
#pragma unroll
    for (int kt = 2 * half; kt < 2 * half + 2; ++kt)
#pragma unroll
      for (int ks = 0; ks < 4; ++ks) kf[kt * 4 + ks] = lds_b128(ka + kt * 4096 + (ks >> 1) * 1024 + (ks & 1) * 512);
    if (half) adv();
  };
  auto readV = [&](int ks) {
    unsigned aE = vbE + slot, aO = vbO + slot;
    asm volatile("" : "+v"(aE), "+v"(aO));
#pragma unroll
    for (int d = 0; d < 8; ++d) {
      const unsigned base = ((d & 1) ? aO : aE) + (d >> 1) * 512;
      vf[d * 2 + ks] = cat8(lds_tr(base + (4 * ks) * 2048), lds_tr(base + (4 * ks + 2) * 2048));
    }
    if (ks) adv();
  };
  constexpr float OFF = K1_OFF;
  f32x4 o[8][QT], negm[QT], lacc[QT];
  const u32x4 onesu = {0x3f803f80u, 0x3f803f80u, 0x3f803f80u, 0x3f803f80u};
  const bf16x8 ones = __builtin_bit_cast(bf16x8, onesu);
#pragma unroll
  for (int u = 0; u < QT; ++u) {
    negm[u] = f32x4{-OFF, -OFF, -OFF, -OFF}; lacc[u] = f32x4{0.f, 0.f, 0.f, 0.f};
#pragma unroll
    for (int d = 0; d < 8; ++d) o[d][u] = f32x4{0.f, 0.f, 0.f, 0.f};
  }
  f32x4 s[4][QT];
  bf16x8 pf[QT][2];
  auto qk = [&](int half) {
#pragma unroll
    for (int kt = 2 * half; kt < 2 * half + 2; ++kt)
#pragma unroll
      for (int ks = 0; ks < 4; ++ks)
#pragma unroll
        for (int u = 0; u < QT; ++u) s[kt][u] = mfma16(kf[kt * 4 + ks], qf[u][ks], ks == 0 ? negm[u] : s[kt][u]);
  };
  auto pv = [&](int ks) {
#pragma unroll
    for (int d = 0; d < 8; ++d)
#pragma unroll
      for (int u = 0; u < QT; ++u) o[d][u] = mfma16(vf[d * 2 + ks], pf[u][ks], o[d][u]);
#pragma unroll
    for (int u = 0; u < QT; ++u) lacc[u] = mfma16(ones, pf[u][ks], lacc[u]);
  };
  auto makeP = [&]() -> unsigned {
    unsigned orr = 0;
#pragma unroll
    for (int u = 0; u < QT; ++u)
#pragma unroll
      for (int ks = 0; ks < 2; ++ks) {
        u32x4 w;
#pragma unroll
        for (int e = 0; e < 4; ++e) {
          const f32x4& sv = s[2 * ks + (e >> 1)][u];
          w[e] = cvtpk(__builtin_amdgcn_exp2f(sv[2 * (e & 1)]), __builtin_amdgcn_exp2f(sv[2 * (e & 1) + 1]));
        }
        orr |= w[0] | w[1];
        orr |= w[2] | w[3];
        pf[u][ks] = __builtin_bit_cast(bf16x8, w);
      }
    return orr;
  };
  readK(0);
  unsigned fl = 3u;
#pragma clang loop unroll(disable)
  for (int t = 0; t < 8; ++t) {
    SBAR();
    readK(1);
    qk(0);
#pragma unroll
    for (int k = 0; k < 8; ++k) {
      __builtin_amdgcn_sched_group_barrier(0x100, 1, 0);
      __builtin_amdgcn_sched_group_barrier(0x008, QT, 0);
    }
    SBAR();
    if (rfl(fl) < (unsigned)(2 * t + 2)) lds_wait_ge(aLAND, (unsigned)(2 * t + 2));
    SBAR();
    readV(0);
    fl = *reinterpret_cast<volatile LAS unsigned*>(aLAND);
    qk(1);
#pragma unroll
    for (int k = 0; k < 8; ++k) {
      __builtin_amdgcn_sched_group_barrier(0x008, QT, 0);
      __builtin_amdgcn_sched_group_barrier(0x100, 2, 0);
    }
    __builtin_amdgcn_sched_group_barrier(0x100, 1, 0);
    SBAR();
    lds_pub(myp, (unsigned)(2 * t + 1));
    refill(2 * t);
    SBAR();
    bool slow = (t == 0);
    if (!slow) slow = __any((makeP() & 0x40004000u) != 0u);
    if (slow) {
#pragma unroll
      for (int u = 0; u < QT; ++u) {
        float a = max3f(s[0][u][0], s[0][u][1], s[0][u][2]), b2 = max3f(s[0][u][3], s[1][u][0], s[1][u][1]);
        a = max3f(a, s[1][u][2], s[1][u][3]); b2 = max3f(b2, s[2][u][0], s[2][u][1]);
        a = max3f(a, s[2][u][2], s[2][u][3]); b2 = max3f(b2, s[3][u][0], s[3][u][1]);
        a = max3f(a, s[3][u][2], s[3][u][3]);
        float dl = xmax_g(fmaxf(a, b2)) + OFF;
        if (t != 0) dl = fmaxf(dl, 0.f);
        const float alpha = __builtin_amdgcn_exp2f(-dl);
#pragma unroll
        for (int kt = 0; kt < 4; ++kt) s[kt][u] -= dl;
        negm[u] -= dl;
        if (t != 0) {
          lacc[u] *= alpha;
#pragma unroll
          for (int d = 0; d < 8; ++d) o[d][u] *= alpha;
        }
      }
      (void)makeP();
    }
    SBAR();
    readV(1);
    pv(0);
#pragma unroll
    for (int k = 0; k < 8; ++k) {
      __builtin_amdgcn_sched_group_barrier(0x100, 2, 0);
      __builtin_amdgcn_sched_group_barrier(0x008, QT, 0);
    }
    __builtin_amdgcn_sched_group_barrier(0x008, QT, 0);
    SBAR();
    {
      const unsigned need = (unsigned)(t < 7 ? 2 * t + 3 : 16);
      if (rfl(fl) < need) lds_wait_ge(aLAND, need);
    }
    SBAR();
    readK(0);
    fl = *reinterpret_cast<volatile LAS unsigned*>(aLAND);
    pv(1);
#pragma unroll
    for (int k = 0; k < 9; ++k) {
      __builtin_amdgcn_sched_group_barrier(0x008, QT, 0);
      __builtin_amdgcn_sched_group_barrier(0x100, 1, 0);
    }
    SBAR();
    lds_pub(myp, (unsigned)(2 * t + 2));
    refill(2 * t + 1);
  }
#pragma unroll
  for (int u = 0; u < QT; ++u) {
    const float inv = 1.0f / lacc[u][0];
#pragma unroll
    for (int s2 = 0; s2 < 4; ++s2)
      *reinterpret_cast<u32x4*>(op + (size_t)u * 131072 + s2 * 1024) = pack_pair(o[2 * s2][u] * inv, o[2 * s2 + 1][u] * inv);
  }
}

DI void q_stage0(const K1Ctx& c) {
  const int lane = c.lane, wave = c.wave;
  const int g = lane >> 4, qq = (lane >> 2) & 3, p = lane & 3;
  const unsigned r8 = (lane >> 2) & 7;
  const __amdgpu_buffer_rsrc_t rX = __builtin_amdgcn_make_buffer_rsrc((void*)c.xg, 0, SQ * DM * 2, 0x00020000);
  const __amdgpu_buffer_rsrc_t rW = __builtin_amdgcn_make_buffer_rsrc((void*)c.wg, 0, (unsigned)((size_t)DM * HD * 2 - (size_t)c.hp * 512), 0x00020000);
  const unsigned vw = (qq * 8192 + g * 64 + p * 16) ^ ((wave & 1) ? 32u : 0u);
  const unsigned vx = (r8 * 2048 + (lane >> 5) * 64 + p * 16) ^ ((wave & 1) ? 32u : 0u);
  const unsigned so = (unsigned)(64 * c.rot + 8 * wave) * 8192u, ld = c.lds0 + (unsigned)wave * 4096u, sx = (unsigned)c.rot * 128u;
  dma16d(rW, vw, so, ld); dma16d(rW, vw, so + 256u, ld + 1024u);
  dma16d(rW, vw, so + 4u * 8192u, ld + 2048u); dma16d(rW, vw, so + 4u * 8192u + 256u, ld + 3072u);
  dma16d(rX, vx, sx + (unsigned)wave * 16384u, c.lds0 + 32768u + (unsigned)wave * 1024u);
  if (wave < 6) dma16d(rX, vx, sx + (unsigned)(wave + 8) * 16384u, c.lds0 + 32768u + (unsigned)(wave + 8) * 1024u);
}

template <int QT>
DI void phase_q(const K1Ctx& c, f32x4 (&acc)[8][QT]) {
  const int lane = c.lane, wave = c.wave, hh = c.hh, w3 = c.w3;
  const unsigned lds0 = c.lds0;
  char* ows = c.ows; (void)ows;
  const int i = lane & 15, g = lane >> 4, qq = (lane >> 2) & 3, p = lane & 3;
  const unsigned wb = lds0 + hh * 1024 + g * 4096 + qq * 64 + p * 8;
  const unsigned wE0 = wb + ((g & 1) * 32), wO0 = wb + (((g & 1) ^ 1) * 32);
  const unsigned xa0 = lds0 + 32768 + w3 * 4096 + (i >> 3) * 1024 + (i & 7) * 64 + ((g ^ ((i >> 3) << 1)) * 16);
  const unsigned aLD = lds0 + L_FLAG + 64, aRD = lds0 + L_FLAG + 96;
  const unsigned gLD = aLD + 4u * (unsigned)(lane & 1);
  const unsigned aPR = aRD + 4u * (unsigned)(wave ^ 4);
#pragma unroll
  for (int t = 0; t < 8; ++t)
#pragma unroll
    for (int u = 0; u < QT; ++u) acc[t][u] = f32x4{0.f, 0.f, 0.f, 0.f};
  bf16x8 a0[8], b0[QT], a1[8], b1[QT];
  unsigned tj = 0; (void)tj;
  q_stage0(c);
  WAIT_VM(0);
  BAR();
  STAMP(2);
  q_read<QT>(a0, b0, wE0, wO0, xa0);
  __builtin_amdgcn_s_waitcnt(0xC07F);
  SBAR();
  q_read<QT>(a1, b1, wE0 + 16384, wO0 + 16384, xa0 + 512);
  q_mma<QT>(acc, a0, b0);
  q_sched<QT>();
  SBAR();
  __builtin_amdgcn_s_waitcnt(0xC07F);
  lds_pub(aRD + 4u * (unsigned)wave, 1u);
  lds_wait_ge(gLD, 2u);
  __builtin_amdgcn_s_waitcnt(0xC07F);
  unsigned ds = DSZ;
  for (int j = 1; j < 16; ++j) {
    SBAR();
    q_read<QT>(a0, b0, wE0 + ds, wO0 + ds, xa0 + ds);
    q_mma<QT>(acc, a1, b1);
    q_sched<QT>();
    SBAR();
    __builtin_amdgcn_s_waitcnt(0xC07F);
    SBAR();
    q_read<QT>(a1, b1, wE0 + ds + 16384, wO0 + ds + 16384, xa0 + ds + 512);
    const unsigned fld = *reinterpret_cast<volatile LAS unsigned*>(gLD);
    unsigned fpr = 0;
    if (QT == 2 && K1_DYNP) fpr = *reinterpret_cast<volatile LAS unsigned*>(aPR);
    q_mma<QT>(acc, a0, b0);
    q_sched<QT>();
    SBAR();
    __builtin_amdgcn_s_waitcnt(0xC07F);
    lds_pub(aRD + 4u * (unsigned)wave, (unsigned)(j + 1));
    if (QT == 2 && K1_DYNP) {
      if (rfl(fpr) >= (unsigned)(j + 1)) __builtin_amdgcn_s_setprio(1); else __builtin_amdgcn_s_setprio(0);
    }
    const unsigned need = (unsigned)(j < 15 ? j + 2 : 16);
    if (!__all(fld >= need)) {
      lds_wait_ge(gLD, need);
      if (QT == 2 && K1_DYNP == 2) __builtin_amdgcn_s_setprio(1);
    }
    ds = (ds == 2 * DSZ) ? 0u : ds + DSZ;
  }
  q_mma<QT>(acc, a1, b1);
  __builtin_amdgcn_s_setprio(0);
}

DI void phase_q_loader(const K1Ctx& c, unsigned vk0, unsigned vv) {
  const int lane = c.lane, hh = c.hh;
  const unsigned lds0 = c.lds0;
  char* ows = c.ows; (void)ows;
  const int g = lane >> 4, qq = (lane >> 2) & 3, p = lane & 3;
  const unsigned r8 = (lane >> 2) & 7;
  const unsigned aLD = lds0 + L_FLAG + 64 + 4u * (unsigned)hh, aRD = lds0 + L_FLAG + 96;
  const int rl = lane & 7;
  const unsigned gRD = aRD + 4u * (unsigned)(rl == 3 ? 0 : (rl == 6 ? 7 : rl));
  const __amdgpu_buffer_rsrc_t rX = __builtin_amdgcn_make_buffer_rsrc((void*)c.xg, 0, SQ * DM * 2, 0x00020000);
  const __amdgpu_buffer_rsrc_t rW = __builtin_amdgcn_make_buffer_rsrc((void*)(c.wg - 4096), 0, (unsigned)((size_t)DM * HD * 2 - (size_t)c.hp * 512) + 4096u, 0x00020000);
  const __amdgpu_buffer_rsrc_t rK = __builtin_amdgcn_make_buffer_rsrc((void*)c.kg, 0, SK * DH * 2, 0x00020000);
  const __amdgpu_buffer_rsrc_t rV = __builtin_amdgcn_make_buffer_rsrc((void*)c.vg, 0, SK * DH * 2, 0x00020000);
  const unsigned vwa = qq * 8192 + g * 64 + p * 16 + 4096u, vwb = (qq * 8192 + g * 64 + (p ^ 2) * 16) + 4096u;
  const unsigned vxa = (r8 * 2048 + (lane >> 5) * 64 + p * 16) ^ (hh ? 32u : 0u), vxb = vxa ^ 32u;
  const unsigned vk1 = vk0 ^ 32u;
  constexpr unsigned D1 = 256u - 1024u, D2 = 4u * 8192u - 2048u, D3 = 4u * 8192u + 256u - 3072u, DX = 16384u - 1024u;
  const unsigned wa1 = vwa + D1, wa2 = vwa + D2, wa3 = vwa + D3, wb1 = vwb + D1, wb2 = vwb + D2, wb3 = vwb + D3;
  const unsigned x1 = vxb + DX, x2 = vxa + 2 * DX, x3 = vxb + 3 * DX;
  auto issueA = [&](int j, unsigned ds) {
    j = (j & ~(K1_WIN - 1)) | ((j + c.rot) & (K1_WIN - 1));
    const unsigned so = (unsigned)(64 * j + 32 * hh) * 8192u, ld = lds0 + ds + (unsigned)hh * 16384u;
    dma16x4(rW, vwa, wa1, wa2, wa3, so, ld);
    dma16x4(rW, vwb, wb1, wb2, wb3, so + 8u * 8192u, ld + 4096u);
    dma16x4(rW, vwa, wa1, wa2, wa3, so + 16u * 8192u, ld + 8192u);
  };
  auto issueB = [&](int j, unsigned ds) {
    j = (j & ~(K1_WIN - 1)) | ((j + c.rot) & (K1_WIN - 1));
    const unsigned so = (unsigned)(64 * j + 32 * hh) * 8192u, ld = lds0 + ds + (unsigned)hh * 16384u;
    dma16x4(rW, vwb, wb1, wb2, wb3, so + 24u * 8192u, ld + 12288u);
    const unsigned sx = (unsigned)j * 128u + (unsigned)(7 * hh) * 16384u, lx = lds0 + ds + 32768u + (unsigned)(7 * hh) * 1024u;
    dma16x4(rX, vxa, x1, x2, x3, sx, lx);
    dma16x3(rX, vxa, x1, x2, sx + 4u * 16384u, lx + 4096u);
  };
  const int bd = c.bd;
  u32x4 kv[48];
  __builtin_amdgcn_s_setprio(K1_LPRIO);
  q_stage0(c);
  if (hh == 0 && lane < 32) reinterpret_cast<volatile LAS unsigned*>(lds0 + L_FLAG)[lane] = 0u;
  WAIT_VM(0);
  BAR();
  const __amdgpu_buffer_rsrc_t rS = __builtin_amdgcn_make_buffer_rsrc((void*)SEQ, 0, 256, 0x00020000);
  issueA(1, DSZ); issueB(1, DSZ);
  dma_flag(rS, 2u, aLD);
  unsigned dn = 2 * DSZ;
  int j = 0;
  unsigned ta = 0, tb = 0, tc = 0; (void)ta; (void)tb; (void)tc;
#define LT(v) do {} while (0)
  auto run = [&](int jend) {
#pragma clang loop unroll(disable)
    for (; j <= jend; ++j) {
      LT(ta);
      if (j >= 1) lds_wait_ge(gRD, (unsigned)j);
      LT(tb);
      issueA(j + 2, dn);
      issueB(j + 2, dn);
      dma_flag(rS, (unsigned)(j + 3), aLD);
      LT(tc);
      dn = (dn == 2 * DSZ) ? 0u : dn + DSZ;
    }
  };
  run(K1_JB0 + bd);
#pragma unroll
  for (int k = 0; k < 16; ++k) kv[k] = __builtin_amdgcn_raw_buffer_load_b128(rK, (k & 2) ? vk1 : vk0, (k >> 1) * 2048 + (k & 1) * 128, 2);
  run(K1_JB1 + bd);
#pragma unroll
  for (int k = 0; k < 16; ++k) kv[16 + k] = __builtin_amdgcn_raw_buffer_load_b128(rV, vv, (k >> 1) * 2048 + (k & 1) * 128, 2);
  run(K1_JB2 + bd);
#pragma unroll
  for (int k = 0; k < 16; ++k) kv[32 + k] = __builtin_amdgcn_raw_buffer_load_b128(rK, (k & 2) ? vk1 : vk0, 16384 + (k >> 1) * 2048 + (k & 1) * 128, 2);
  run(13);
  lds_wait_ge(gRD, 14u);
  {
    const unsigned a = lds0 + ring_slot(hh, 0) + lane * 16;
#pragma unroll
    for (int k = 0; k < 16; ++k) *reinterpret_cast<LAS u32x4*>(a + k * 1024) = kv[k];
  }
  lds_wait_ge(gRD, 15u);
  {
    const unsigned a1 = lds0 + ring_slot(hh, 1) + lane * 16, a2 = lds0 + ring_slot(hh, 2) + lane * 16;
#pragma unroll
    for (int k = 0; k < 16; ++k) *reinterpret_cast<LAS u32x4*>(a1 + k * 1024) = kv[16 + k];
#pragma unroll
    for (int k = 0; k < 16; ++k) *reinterpret_cast<LAS u32x4*>(a2 + k * 1024) = kv[32 + k];
  }
  __builtin_amdgcn_s_setprio(0);
}

__global__ __launch_bounds__(512, 2) __attribute__((amdgpu_waves_per_eu(2, 2))) void k1_qattn(
    const __hip_bfloat16* __restrict__ xA, const __hip_bfloat16* __restrict__ kA, const __hip_bfloat16* __restrict__ vA,
    const __hip_bfloat16* __restrict__ wqA, const __hip_bfloat16* __restrict__ xB, const __hip_bfloat16* __restrict__ kB,
    const __hip_bfloat16* __restrict__ vB, const __hip_bfloat16* __restrict__ wqB, __hip_bfloat16* __restrict__ ows) {
  extern __shared__ __attribute__((aligned(16))) char shm[];
  const unsigned lds0 = (unsigned)(uintptr_t)(LAS char*)shm;
  const int tid = threadIdx.x, lane = tid & 63;
  const int wave = (int)rfl((unsigned)tid >> 6);
  const int hh = wave >> 2, w3 = (wave & 3) ^ ((wave >= 6) ? 1 : 0);
  const int bid = blockIdx.x, xcd = bid & 7, jj = bid >> 3;
  const int side = xcd >> 2, b = jj & 7, hpl = jj >> 3, hp = (xcd & 3) * 4 + hpl;
  K1Ctx c;
  c.xg = (const char*)(side ? xB : xA) + (size_t)b * SQ * DM * 2;
  c.wg = (const char*)(side ? wqB : wqA) + (size_t)hp * 512;
  c.kg = (const char*)(side ? kB : kA) + (size_t)(b * NH + 2 * hp + hh) * SK * DH * 2;
  c.vg = (const char*)(side ? vB : vA) + (size_t)(b * NH + 2 * hp + hh) * SK * DH * 2;
  c.lds0 = lds0; c.wave = wave; c.hh = hh; c.w3 = w3; c.lane = lane; c.b = b; c.hpl = hpl; c.hp = hp; c.jj = jj; c.ows = (char*)ows;
  c.bd = ((jj >> 2) & 1) * K1_JBD;
  c.rot = (K1_WIN == 4) ? ((b + hpl) & 3) : (K1_WIN == 8) ? ((b + 2 * hpl) & 7) : (K1_WIN == 2) ? ((b + hpl) & 1) : 0;
  if (tid == 0) *reinterpret_cast<unsigned*>((char*)ows + WS_FLAG + (size_t)bid * 128) = 0u;
  const unsigned r8 = (lane >> 2) & 7;
  const unsigned vk0 = r8 * 256 + (lane >> 5) * 64 + (lane & 3) * 16;
  const unsigned vv = r8 * 256 + (lane >> 5) * 64 + ((lane & 3) ^ ((r8 >> 2) << 1)) * 16;
  char* op = (char*)ows + ((size_t)((side * NB + b) * 7 + 2 * w3) * 128 + (2 * hp + hh) * 4) * 1024 + lane * 16;
  const unsigned qx = lds0 + L_QX + (unsigned)hh * 4096u + lane * 16;
  STAMP(0); STAMPC(8);
  if (w3 < 3) {
    bf16x8 qf[2][4];
    if (w3 < 2) {
      f32x4 acc[8][2];
      phase_q<2>(c, acc);
#pragma unroll
      for (int s = 0; s < 4; ++s)
#pragma unroll
        for (int u = 0; u < 2; ++u) qf[u][s] = __builtin_bit_cast(bf16x8, pack_pair(acc[2 * s][u] * SM_SC, acc[2 * s + 1][u] * SM_SC));
    } else {
      f32x4 acc[8][3];
      phase_q<3>(c, acc);
#pragma unroll
      for (int s = 0; s < 4; ++s) {
#pragma unroll
        for (int u = 0; u < 2; ++u) qf[u][s] = __builtin_bit_cast(bf16x8, pack_pair(acc[2 * s][u] * SM_SC, acc[2 * s + 1][u] * SM_SC));
        *reinterpret_cast<LAS u32x4*>(qx + s * 1024) = pack_pair(acc[2 * s][2] * SM_SC, acc[2 * s + 1][2] * SM_SC);
      }
    }
    BAR();
    STAMP(1); STAMPC(9);
    if (hh == 0) phase_a<2, 5>(c, qf, op, vk0, vv);
    else phase_a<2, 4>(c, qf, op, vk0, vv);
  } else {
    phase_q_loader(c, vk0, vv);
    BAR();
    bf16x8 qf[1][4];
#pragma unroll
    for (int s = 0; s < 4; ++s) qf[0][s] = lds_b128(qx + s * 1024);
    if (hh == 0) phase_a<1, 5>(c, qf, op, vk0, vv);
    else phase_a<1, 4>(c, qf, op, vk0, vv);
  }
  STAMP(6); STAMPC(11);
}

constexpr int NS2 = 9;
constexpr unsigned L2_FLAG = NS2 * SLOT;
constexpr unsigned K2_LDS = NS2 * SLOT + 1024;
#define K2_WNT 1
typedef __attribute__((address_space(1))) unsigned gu32;

__global__ __launch_bounds__(512, 2) __attribute__((amdgpu_waves_per_eu(2, 2))) void k2_oproj(
    __hip_bfloat16* __restrict__ ows, const __hip_bfloat16* __restrict__ woA, const __hip_bfloat16* __restrict__ woB,
    __hip_bfloat16* __restrict__ out) {
  extern __shared__ __attribute__((aligned(16))) char shm[];
  const unsigned lds0 = (unsigned)(uintptr_t)(LAS char*)shm;
  const int tid = threadIdx.x, lane = tid & 63;
  const int wave = (int)rfl((unsigned)tid >> 6);
  const int bid = blockIdx.x, xcd = bid & 7, jj = bid >> 3;
  const int side = xcd >> 2, kq = xcd & 3, bp = jj & 3, nt = jj >> 2;
  const int i = lane & 15, g = lane >> 4, qq = (lane >> 2) & 3, p = lane & 3;
  const unsigned aFULL = lds0 + L2_FLAG, aPROG = lds0 + L2_FLAG + 64;
  const int tile = (side * 4 + bp) * 8 + nt;
  gu32* flags = (gu32*)((char*)ows + WS_FLAG + (size_t)tile * 512);
  STAMP2(0);
  f32x4 acc[8][2];
  if (wave == 7) {
    __builtin_amdgcn_s_setprio(3);
    if (lane < 32) reinterpret_cast<volatile LAS unsigned*>(lds0 + L2_FLAG)[lane] = 0u;
    const char* wg = (const char*)(side ? woB : woA) + (size_t)kq * 1024 * 2048 + (size_t)nt * 256;
    const __amdgpu_buffer_rsrc_t rW = __builtin_amdgcn_make_buffer_rsrc((void*)wg, 0, (unsigned)(1024 * 2048 - nt * 256), 0x00020000);
    const unsigned vw0 = qq * 2048 + g * 64 + p * 16, vw1 = vw0 ^ 32u;
    auto issue = [&](int n, unsigned slot) {
#pragma unroll
      for (int pc = 0; pc < 16; ++pc) {
        if (K2_WNT) dma16nt(rW, ((pc >> 1) & 1) ? vw1 : vw0, ((unsigned)n * 64u + (unsigned)pc * 4u) * 2048u, lds0 + slot + pc * 1024u);
        else dma16(rW, ((pc >> 1) & 1) ? vw1 : vw0, ((unsigned)n * 64u + (unsigned)pc * 4u) * 2048u, lds0 + slot + pc * 1024u);
      }
    };
    issue(0, 0);
    BAR();
    issue(1, SLOT); issue(2, 2 * SLOT);
    unsigned slot = 3 * SLOT;
    for (int n = 3; n < 16; ++n) {
      WAIT_VM(32);
      lds_pub(aFULL, (unsigned)(n - 2));
      if (n >= NS2) lds_wait_ge(aPROG + 4u * (unsigned)(lane < 7 ? lane : 6), (unsigned)(n - NS2 + 1));
      issue(n, slot);
      slot = (slot == (NS2 - 1) * SLOT) ? 0u : slot + SLOT;
    }
    WAIT_VM(32);
    lds_pub(aFULL, 14u);
    WAIT_VM(16);
    lds_pub(aFULL, 15u);
    WAIT_VM(0);
    lds_pub(aFULL, 16u);
  } else {
    const char* og = (const char*)ows + ((size_t)((side * NB + 2 * bp) * 7 + wave) * 128 + kq * 32) * 1024;
    const __amdgpu_buffer_rsrc_t rO0 = __builtin_amdgcn_make_buffer_rsrc((void*)og, 0, 32768, 0x00020000);
    const __amdgpu_buffer_rsrc_t rO1 = __builtin_amdgcn_make_buffer_rsrc((void*)(og + 7 * 131072), 0, 32768, 0x00020000);
    const unsigned wb = lds0 + g * 2048 + qq * 64 + p * 8;
    const unsigned wE = wb + (g & 1) * 32, wO = wb + ((g & 1) ^ 1) * 32;
    u32x4 of[8][2];
#pragma unroll
    for (int k = 0; k < 8; ++k) {
      of[k][0] = __builtin_amdgcn_raw_buffer_load_b128(rO0, lane * 16, k * 1024, 0);
      of[k][1] = __builtin_amdgcn_raw_buffer_load_b128(rO1, lane * 16, k * 1024, 0);
    }
#pragma unroll
    for (int t = 0; t < 8; ++t) { acc[t][0] = f32x4{0.f, 0.f, 0.f, 0.f}; acc[t][1] = f32x4{0.f, 0.f, 0.f, 0.f}; }
    bf16x8 af[2][8];
    auto readA = [&](bf16x8 (&a)[8], unsigned sl) {
      unsigned aE = wE + sl, aO = wO + sl;
      asm volatile("" : "+v"(aE), "+v"(aO));
#pragma unroll
      for (int t = 0; t < 8; ++t) {
        const unsigned ad = ((t & 1) ? aO : aE) + (t >> 1) * 256;
        a[t] = cat8(lds_tr(ad), lds_tr(ad + 1024));
      }
    };
    BAR();
    lds_wait_ge(aFULL, 1u);
    readA(af[0], 0u);
    unsigned slot = 0;
    for (int so = 0; so < 16; so += 4) {
#pragma unroll
      for (int rr = 0; rr < 4; ++rr) {
        const int st = so + rr;
        const unsigned nslot = (slot == (NS2 - 1) * SLOT) ? 0u : slot + SLOT;
        const unsigned need = (unsigned)(st + 2 > 16 ? 16 : st + 2);
        unsigned fv = 0;
#pragma unroll
        for (int hs = 0; hs < 2; ++hs) {
          __builtin_amdgcn_s_waitcnt(0xC07F);
          if (hs == 1) lds_pub(aPROG + 4u * (unsigned)wave, (unsigned)(st + 1));
          SBAR();
          if (hs == 0) fv = *reinterpret_cast<volatile LAS unsigned*>(aFULL);
          readA(af[hs ^ 1], hs == 0 ? slot + 8192u : nslot);
          const int k = rr * 2 + hs;
#pragma unroll
          for (int t = 0; t < 8; ++t) {
            acc[t][0] = mfma16(af[hs][t], __builtin_bit_cast(bf16x8, of[k][0]), acc[t][0]);
            acc[t][1] = mfma16(af[hs][t], __builtin_bit_cast(bf16x8, of[k][1]), acc[t][1]);
          }
          of[k][0] = __builtin_amdgcn_raw_buffer_load_b128(rO0, lane * 16, ((st + 4) * 2 + hs) * 1024, 0);
          of[k][1] = __builtin_amdgcn_raw_buffer_load_b128(rO1, lane * 16, ((st + 4) * 2 + hs) * 1024, 0);
          if (hs == 0) __builtin_amdgcn_sched_group_barrier(0x100, 1, 0);
#pragma unroll
          for (int k2 = 0; k2 < 8; ++k2) {
            __builtin_amdgcn_sched_group_barrier(0x008, 1, 0);
            __builtin_amdgcn_sched_group_barrier(0x100, 2, 0);
          }
          __builtin_amdgcn_sched_group_barrier(0x008, 8, 0);
          __builtin_amdgcn_sched_group_barrier(0x020, 2, 0);
          SBAR();
          if (hs == 0 && rfl(fv) < need) lds_wait_ge(aFULL, need);
        }
        slot = nslot;
      }
    }
    STAMP2(1);
    const char* pb = (const char*)ows + WS_PART + (size_t)tile * (16 * 7 * 4096);
    const __amdgpu_buffer_rsrc_t rP = __builtin_amdgcn_make_buffer_rsrc((void*)pb, 0, 16 * 7 * 4096, 0x00020000);
#pragma unroll
    for (int t = 0; t < 4; ++t) {
      if (t != kq) {
        const unsigned off = (unsigned)(((t * 4 + kq) * 7 + wave) * 4096);
#pragma unroll
        for (int e = 0; e < 4; ++e)
          __builtin_amdgcn_raw_buffer_store_b128(__builtin_bit_cast(u32x4, acc[2 * t + (e >> 1)][e & 1]), rP, lane * 16, off + e * 1024, 16);
      }
    }
  }
  WAIT_VM(0);
  BAR();
  if (wave == 7) {
    if (lane == 0) __hip_atomic_store(flags + kq * 32, 1u, __ATOMIC_RELAXED, __HIP_MEMORY_SCOPE_AGENT);
    for (int it = 0; it < (1 << 20); ++it) {
      const unsigned v = __hip_atomic_load(flags + (lane & 3) * 32, __ATOMIC_RELAXED, __HIP_MEMORY_SCOPE_AGENT);
      if (__all(v != 0u)) break;
      __builtin_amdgcn_s_sleep(2);
    }
    asm volatile("" ::: "memory");
  }
  BAR();
  STAMP2(3);
  if (wave < 7) {
    const char* pb = (const char*)ows + WS_PART + (size_t)tile * (16 * 7 * 4096);
    const __amdgpu_buffer_rsrc_t rP = __builtin_amdgcn_make_buffer_rsrc((void*)pb, 0, 16 * 7 * 4096, 0x00020000);
    f32x4 sum[4];
#pragma unroll
    for (int e = 0; e < 4; ++e)
      sum[e] = (kq == 0) ? acc[0 + (e >> 1)][e & 1] : (kq == 1) ? acc[2 + (e >> 1)][e & 1] : (kq == 2) ? acc[4 + (e >> 1)][e & 1] : acc[6 + (e >> 1)][e & 1];
    u32x4 pv[3][4];
#pragma unroll
    for (int k = 0; k < 3; ++k) {
      const int src = (kq + 1 + k) & 3;
      const unsigned off = (unsigned)(((kq * 4 + src) * 7 + wave) * 4096);
#pragma unroll
      for (int e = 0; e < 4; ++e) pv[k][e] = __builtin_amdgcn_raw_buffer_load_b128(rP, lane * 16, off + e * 1024, 16);
    }
#pragma unroll
    for (int k = 0; k < 3; ++k)
#pragma unroll
      for (int e = 0; e < 4; ++e) sum[e] += __builtin_bit_cast(f32x4, pv[k][e]);
    char* op = (char*)out + ((size_t)(2 * bp * SQ + 16 * wave + i) * (2 * DM) + side * DM + nt * 128 + kq * 32 + 8 * g) * 2;
    *reinterpret_cast<u32x4*>(op) = pack_pair(sum[0], sum[2]);
    *reinterpret_cast<u32x4*>(op + (size_t)SQ * (2 * DM) * 2) = pack_pair(sum[1], sum[3]);
  }
  STAMP2(2);
}

extern "C" void kernel_launch(void* const* d_in, const int* in_sizes, int n_in, void* d_out, int out_size, void* d_ws, size_t ws_size,
                              hipStream_t stream) {
  (void)in_sizes; (void)n_in; (void)out_size; (void)ws_size;
  typedef const __hip_bfloat16* cp;
  (void)hipFuncSetAttribute((const void*)k1_qattn, hipFuncAttributeMaxDynamicSharedMemorySize, (int)K1_LDS);
  (void)hipFuncSetAttribute((const void*)k2_oproj, hipFuncAttributeMaxDynamicSharedMemorySize, (int)K2_LDS);
  __hip_bfloat16* ows = (__hip_bfloat16*)d_ws;
  k1_qattn<<<256, 512, K1_LDS, stream>>>((cp)d_in[0], (cp)d_in[1], (cp)d_in[2], (cp)d_in[3], (cp)d_in[5], (cp)d_in[6], (cp)d_in[7],
                                         (cp)d_in[8], ows);
  k2_oproj<<<256, 512, K2_LDS, stream>>>(ows, (cp)d_in[4], (cp)d_in[9], (__hip_bfloat16*)d_out);
}
```

```cpp
#include <hip/hip_runtime.h>
#include <hip/hip_bf16.h>
#include <stdint.h>

typedef __bf16 bf16x2 __attribute__((ext_vector_type(2)));
typedef __bf16 bf16x4 __attribute__((ext_vector_type(4)));
typedef __bf16 bf16x8 __attribute__((ext_vector_type(8)));
typedef float f32x2 __attribute__((ext_vector_type(2)));
typedef float f32x4 __attribute__((ext_vector_type(4)));
typedef unsigned u32x4 __attribute__((ext_vector_type(4)));
#define LAS __attribute__((address_space(3)))
#define DI __device__ __forceinline__

constexpr size_t WS_PART = (size_t)16 << 20, WS_FLAG = (size_t)48 << 20;
constexpr int NB = 8, SQ = 112, DM = 1024, NH = 32, SK = 512, DH = 128, HD = NH * DH;

#define K1_ROT 1
#define K1_PF0 2
#define K1_DEPTH 3

DI unsigned rfl(unsigned x) { return __builtin_amdgcn_readfirstlane(x); }
DI void dma16(__amdgpu_buffer_rsrc_t rsrc, unsigned voff, unsigned soff, unsigned lds) {
  asm volatile("s_mov_b32 m0, %0\n\ts_nop 0\n\tbuffer_load_dwordx4 %1, %2, %3 offen lds"
               :: "s"(lds), "v"(voff), "s"(rsrc), "s"(soff) : "m0", "memory");
}
DI void dma16nt(__amdgpu_buffer_rsrc_t rsrc, unsigned voff, unsigned soff, unsigned lds) {
  asm volatile("s_mov_b32 m0, %0\n\ts_nop 0\n\tbuffer_load_dwordx4 %1, %2, %3 offen nt lds"
               :: "s"(lds), "v"(voff), "s"(rsrc), "s"(soff) : "m0", "memory");
}
#define K1_DNT 0
#define K1_DYNP 2
#define K1_WIN 4
#define DPOL ""
DI void dma16d(__amdgpu_buffer_rsrc_t rsrc, unsigned voff, unsigned soff, unsigned lds) {
  asm volatile("s_mov_b32 m0, %0\n\ts_nop 0\n\tbuffer_load_dwordx4 %1, %2, %3 offen" DPOL " lds"
               :: "s"(lds), "v"(voff), "s"(rsrc), "s"(soff) : "m0", "memory");
}
DI void dma16x4(__amdgpu_buffer_rsrc_t rsrc, unsigned v0, unsigned v1, unsigned v2, unsigned v3, unsigned soff, unsigned lds) {
  asm volatile("s_mov_b32 m0, %0\n\ts_nop 0\n\t"
               "buffer_load_dwordx4 %1, %5, %6 offen" DPOL " lds\n\t"
               "buffer_load_dwordx4 %2, %5, %6 offen offset:1024" DPOL " lds\n\t"
               "buffer_load_dwordx4 %3, %5, %6 offen offset:2048" DPOL " lds\n\t"
               "buffer_load_dwordx4 %4, %5, %6 offen offset:3072" DPOL " lds"
               :: "s"(lds), "v"(v0), "v"(v1), "v"(v2), "v"(v3), "s"(rsrc), "s"(soff) : "m0", "memory");
}
DI void dma16x3(__amdgpu_buffer_rsrc_t rsrc, unsigned v0, unsigned v1, unsigned v2, unsigned soff, unsigned lds) {
  asm volatile("s_mov_b32 m0, %0\n\ts_nop 0\n\t"
               "buffer_load_dwordx4 %1, %4, %5 offen" DPOL " lds\n\t"
               "buffer_load_dwordx4 %2, %4, %5 offen offset:1024" DPOL " lds\n\t"
               "buffer_load_dwordx4 %3, %4, %5 offen offset:2048" DPOL " lds"
               :: "s"(lds), "v"(v0), "v"(v1), "v"(v2), "s"(rsrc), "s"(soff) : "m0", "memory");
}
__device__ const unsigned SEQ[64] = {0, 1, 2, 3, 4, 5, 6, 7, 8, 9, 10, 11, 12, 13, 14, 15, 16, 17, 18, 19, 20, 21, 22, 23, 24, 25, 26, 27, 28, 29, 30, 31,
                                     32, 33, 34, 35, 36, 37, 38, 39, 40, 41, 42, 43, 44, 45, 46, 47, 48, 49, 50, 51, 52, 53, 54, 55, 56, 57, 58, 59, 60, 61, 62, 63};
DI void dma_flag(__amdgpu_buffer_rsrc_t rseq, unsigned val, unsigned lds) {
  asm volatile("s_mov_b32 m0, %0\n\ts_mov_b64 exec, 1\n\tbuffer_load_dword off, %1, %2 lds\n\ts_mov_b64 exec, -1"
               :: "s"(lds), "s"(rseq), "s"(val * 4u) : "m0", "memory");
}
DI void touch4(__amdgpu_buffer_rsrc_t rsrc, unsigned voff, unsigned soff, unsigned lds) {
  asm volatile("s_mov_b32 m0, %0\n\ts_nop 0\n\tbuffer_load_dword %1, %2, %3 offen lds"
               :: "s"(lds), "v"(voff), "s"(rsrc), "s"(soff) : "m0", "memory");
}
DI void touch4nt(__amdgpu_buffer_rsrc_t rsrc, unsigned voff, unsigned soff, unsigned lds) {
  asm volatile("s_mov_b32 m0, %0\n\ts_nop 0\n\tbuffer_load_dword %1, %2, %3 offen nt lds"
               :: "s"(lds), "v"(voff), "s"(rsrc), "s"(soff) : "m0", "memory");
}
#define WAIT_VM(n) asm volatile("s_waitcnt vmcnt(" #n ")" ::: "memory")
#define BAR() do { asm volatile("" ::: "memory"); __builtin_amdgcn_s_waitcnt(0xC07F); asm volatile("s_barrier" ::: "memory"); } while (0)
#define SBAR() __builtin_amdgcn_sched_barrier(0)
DI void lds_pub(unsigned addr, unsigned val) {
  asm volatile("s_mov_b64 exec, 1\n\tds_write_b32 %0, %1\n\ts_mov_b64 exec, -1" :: "v"(addr), "v"(val) : "memory");
}
DI void lds_wait_ge(unsigned addr, unsigned need) {
  unsigned tmp, cnt = 1u << 22;
  asm volatile(
      "LW%=:\n\t"
      "ds_read_b32 %0, %2\n\t"
      "s_waitcnt lgkmcnt(0)\n\t"
      "v_cmp_le_u32 vcc, %3, %0\n\t"
      "s_sub_u32 %1, %1, 1\n\t"
      "s_cbranch_scc1 LE%=\n\t"
      "s_cmp_eq_u64 vcc, -1\n\t"
      "s_cbranch_scc1 LE%=\n\t"
      "s_sleep 1\n\t"
      "s_branch LW%=\n\t"
      "LE%=:"
      : "=&v"(tmp), "+s"(cnt) : "v"(addr), "s"(need) : "memory", "scc", "vcc");
}
DI bf16x8 lds_b128(unsigned a) { return *reinterpret_cast<LAS bf16x8*>(a); }
DI bf16x4 lds_tr(unsigned a) { return __builtin_amdgcn_ds_read_tr16_b64_v4bf16(reinterpret_cast<LAS bf16x4*>(a)); }
DI bf16x8 cat8(bf16x4 a, bf16x4 b) { return __builtin_shufflevector(a, b, 0, 1, 2, 3, 4, 5, 6, 7); }
DI unsigned cvtpk(float lo, float hi) {
  f32x2 v = {lo, hi};
  bf16x2 b = __builtin_convertvector(v, bf16x2);
  return __builtin_bit_cast(unsigned, b);
}
DI f32x4 mfma16(bf16x8 a, bf16x8 b, f32x4 c) { return __builtin_amdgcn_mfma_f32_16x16x32_bf16(a, b, c, 0, 0, 0); }
DI u32x4 pack_pair(f32x4 A, f32x4 B) {
  unsigned a0 = cvtpk(A[0], A[1]), a1 = cvtpk(A[2], A[3]), b0 = cvtpk(B[0], B[1]), b1 = cvtpk(B[2], B[3]);
  auto r0 = __builtin_amdgcn_permlane32_swap(a0, b0, false, false);
  auto s0 = __builtin_amdgcn_permlane16_swap(r0[0], r0[1], false, false);
  auto r1 = __builtin_amdgcn_permlane32_swap(a1, b1, false, false);
  auto s1 = __builtin_amdgcn_permlane16_swap(r1[0], r1[1], false, false);
  u32x4 o = {s0[0], s1[0], s0[1], s1[1]};
  return o;
}
DI float xmax_g(float v) {
  auto r = __builtin_amdgcn_permlane32_swap(__float_as_uint(v), __float_as_uint(v), false, false);
  v = fmaxf(__uint_as_float(r[0]), __uint_as_float(r[1]));
  auto s = __builtin_amdgcn_permlane16_swap(__float_as_uint(v), __float_as_uint(v), false, false);
  return fmaxf(__uint_as_float(s[0]), __uint_as_float(s[1]));
}
DI float xsum_g(float v) {
  auto r = __builtin_amdgcn_permlane32_swap(__float_as_uint(v), __float_as_uint(v), false, false);
  v = __uint_as_float(r[0]) + __uint_as_float(r[1]);
  auto s = __builtin_amdgcn_permlane16_swap(__float_as_uint(v), __float_as_uint(v), false, false);
  return __uint_as_float(s[0]) + __uint_as_float(s[1]);
}

#define STAMP(idx) do {} while (0)
#define STAMP2(idx) do {} while (0)
#define STAMPC(idx) do {} while (0)
#define TACC(acc, last) do {} while (0)
#define TPUT(idx, v) do {} while (0)
DI unsigned long long tnow() { return 0; }
constexpr unsigned SLOT = 16384;
constexpr unsigned DSZ = 47104;
constexpr unsigned L_FLAG = 9 * SLOT;
constexpr unsigned L_QX = L_FLAG + 1024;
constexpr unsigned K1_LDS = L_QX + 8192;
#define K1_TNT 1
#define K1_LPRIO 3
#define K1_JT1 0
#define K1_JT2 5
#define K1_JB0 3
#define K1_JB1 7
#define K1_JB2 11
#define K1_JBD 1

DI void gtouch(const char* p, unsigned lds) {
  if (K1_TNT) asm volatile("s_mov_b32 m0, %0\n\ts_nop 0\n\tglobal_load_lds_dword %1, off nt" :: "s"(lds), "v"(p) : "m0", "memory");
  else asm volatile("s_mov_b32 m0, %0\n\ts_nop 0\n\tglobal_load_lds_dword %1, off" :: "s"(lds), "v"(p) : "m0", "memory");
}
DI unsigned ring_slot(int hh, int r) { return (((hh ? 0x2874u : 0x10653u) >> (4 * r)) & 15u) * SLOT; }

template <int QT>
DI void q_read(bf16x8 (&a)[8], bf16x8 (&b)[QT], unsigned wE, unsigned wO, unsigned xa) {
  asm volatile("" : "+v"(wE), "+v"(wO), "+v"(xa));
#pragma unroll
  for (int u = 0; u < QT; ++u) b[u] = lds_b128(xa + u * 2048);
#pragma unroll
  for (int t = 0; t < 8; ++t) {
    const unsigned base = (t & 1) ? wO : wE;
    a[t] = cat8(lds_tr(base + (t >> 1) * 256), lds_tr(base + (t >> 1) * 256 + 2048));
  }
}
template <int QT>
DI void q_mma(f32x4 (&acc)[8][QT], const bf16x8 (&a)[8], const bf16x8 (&b)[QT]) {
#pragma unroll
  for (int t = 0; t < 8; ++t)
#pragma unroll
    for (int u = 0; u < QT; ++u) acc[t][u] = mfma16(a[t], b[u], acc[t][u]);
}
template <int QT>
DI void q_sched() {
#pragma unroll
  for (int k = 0; k < 10; ++k) {
    __builtin_amdgcn_sched_group_barrier(0x008, 1, 0);
    __builtin_amdgcn_sched_group_barrier(0x100, 2, 0);
  }
  __builtin_amdgcn_sched_group_barrier(0x008, 8 * QT - 10, 0);
}

constexpr float SM_SC = 0.08838834764831845f * 1.4426950408889634f;
struct K1Ctx {
  const char *xg, *wg, *kg, *vg;
  unsigned lds0;
  int wave, hh, w3, lane, b, hpl, hp, jj, bd, rot;
  char* ows;
};

#define K1_KVNT 1
#define KVPOL " nt"
#define KVAUX 2
DI void dma16x4nt(__amdgpu_buffer_rsrc_t rsrc, unsigned v0, unsigned v1, unsigned v2, unsigned v3, unsigned soff, unsigned lds) {
  asm volatile("s_mov_b32 m0, %0\n\ts_nop 0\n\t"
               "buffer_load_dwordx4 %1, %5, %6 offen" KVPOL " lds\n\t"
               "buffer_load_dwordx4 %2, %5, %6 offen offset:1024" KVPOL " lds\n\t"
               "buffer_load_dwordx4 %3, %5, %6 offen offset:2048" KVPOL " lds\n\t"
               "buffer_load_dwordx4 %4, %5, %6 offen offset:3072" KVPOL " lds"
               :: "s"(lds), "v"(v0), "v"(v1), "v"(v2), "v"(v3), "s"(rsrc), "s"(soff) : "m0", "memory");
}
DI float max3f(float a, float b, float c) { return fmaxf(fmaxf(a, b), c); }
#define K1_OFF 16.0f

template <int QT, int NS>
DI void phase_a(const K1Ctx& c, const bf16x8 (&qf)[QT][4], char* op, unsigned vk0, unsigned vv) {
  const int lane = c.lane, w3 = c.w3, hh = c.hh;
  const unsigned lds0 = c.lds0;
  const int i = lane & 15, g = lane >> 4, qq = (lane >> 2) & 3, p = lane & 3;
  const unsigned aLAND = lds0 + L_FLAG + 16u * (unsigned)hh, aPROG = lds0 + L_FLAG + 32u + 16u * (unsigned)hh;
  const unsigned kb = lds0 + (i >> 3) * 2048 + (i & 7) * 64 + ((g ^ ((i >> 3) << 1)) * 16);
  const unsigned vb0 = lds0 + (g >> 1) * 2048 + (4 * (g & 1) + qq) * 64 + p * 8;
  const unsigned vbE = vb0 + (g & 1) * 32, vbO = vb0 + ((g & 1) ^ 1) * 32;
  const unsigned gp = aPROG + 4u * (unsigned)(lane & 3), myp = aPROG + 4u * (unsigned)w3;
  const __amdgpu_buffer_rsrc_t rK = __builtin_amdgcn_make_buffer_rsrc((void*)(c.kg - 1024), 0, SK * DH * 2 + 1024, 0x00020000);
  const __amdgpu_buffer_rsrc_t rV = __builtin_amdgcn_make_buffer_rsrc((void*)(c.vg - 1024), 0, SK * DH * 2 + 1024, 0x00020000);
  const __amdgpu_buffer_rsrc_t rS = __builtin_amdgcn_make_buffer_rsrc((void*)SEQ, 0, 256, 0x00020000);
  const unsigned k0 = vk0 + 1024u, k1 = vk0 + 128u, k2 = (vk0 ^ 32u) + 1024u, k3 = (vk0 ^ 32u) + 128u;
  const unsigned v0 = vv + 1024u, v1 = vv + 128u;
  auto issue = [&](int m, unsigned sl) {
    const unsigned so = (unsigned)(m >> 1) * 16384u;
    const unsigned ld = lds0 + sl;
    if (m & 1) {
#pragma unroll
      for (int k = 0; k < 4; ++k) dma16x4nt(rV, v0, v1, v0, v1, so + k * 4096u, ld + k * 4096u);
    } else {
#pragma unroll
      for (int k = 0; k < 4; ++k) dma16x4nt(rK, k0, k1, k2, k3, so + k * 4096u, ld + k * 4096u);
    }
    dma_flag(rS, (unsigned)(m + 1), aLAND);
  };
  int rp = 0;
  unsigned slot = ring_slot(hh, 0);
  auto adv = [&]() { rp = (rp == NS - 1) ? 0 : rp + 1; slot = ring_slot(hh, rp); };
  int rq = 0;
  auto refill = [&](int m) {
    if (QT == 1) {
      if (m + NS < 16) {
        lds_wait_ge(gp, (unsigned)(m + 1));
        issue(m + NS, ring_slot(hh, rq));
      }
      rq = (rq == NS - 1) ? 0 : rq + 1;
    }
  };
  if (QT == 1) {
    lds_pub(aLAND, 3u);
    issue(3, ring_slot(hh, 3));
    if (NS == 5) issue(4, ring_slot(hh, 4));
  }
  bf16x8 kf[16], vf[16];
  auto readK = [&](int half) {
    unsigned ka = kb + slot;
    asm volatile("" : "+v"(ka));
#pragma unroll
    for (int kt = 2 * half; kt < 2 * half + 2; ++kt)
#pragma unroll
      for (int ks = 0; ks < 4; ++ks) kf[kt * 4 + ks] = lds_b128(ka + kt * 4096 + (ks >> 1) * 1024 + (ks & 1) * 512);
    if (half) adv();
  };
  auto readV = [&](int ks) {
    unsigned aE = vbE + slot, aO = vbO + slot;
    asm volatile("" : "+v"(aE), "+v"(aO));
#pragma unroll
    for (int d = 0; d < 8; ++d) {
      const unsigned base = ((d & 1) ? aO : aE) + (d >> 1) * 512;
      vf[d * 2 + ks] = cat8(lds_tr(base + (4 * ks) * 2048), lds_tr(base + (4 * ks + 2) * 2048));
    }
    if (ks) adv();
  };
  constexpr float OFF = K1_OFF;
  f32x4 o[8][QT], negm[QT], lacc[QT];
  const u32x4 onesu = {0x3f803f80u, 0x3f803f80u, 0x3f803f80u, 0x3f803f80u};
  const bf16x8 ones = __builtin_bit_cast(bf16x8, onesu);
#pragma unroll
  for (int u = 0; u < QT; ++u) {
    negm[u] = f32x4{-OFF, -OFF, -OFF, -OFF}; lacc[u] = f32x4{0.f, 0.f, 0.f, 0.f};
#pragma unroll
    for (int d = 0; d < 8; ++d) o[d][u] = f32x4{0.f, 0.f, 0.f, 0.f};
  }
  f32x4 s[4][QT];
  bf16x8 pf[QT][2];
  auto qk = [&](int half) {
#pragma unroll
    for (int kt = 2 * half; kt < 2 * half + 2; ++kt)
#pragma unroll
      for (int ks = 0; ks < 4; ++ks)
#pragma unroll
        for (int u = 0; u < QT; ++u) s[kt][u] = mfma16(kf[kt * 4 + ks], qf[u][ks], ks == 0 ? negm[u] : s[kt][u]);
  };
  auto pv = [&](int ks) {
#pragma unroll
    for (int d = 0; d < 8; ++d)
#pragma unroll
      for (int u = 0; u < QT; ++u) o[d][u] = mfma16(vf[d * 2 + ks], pf[u][ks], o[d][u]);
#pragma unroll
    for (int u = 0; u < QT; ++u) lacc[u] = mfma16(ones, pf[u][ks], lacc[u]);
  };
  auto makeP = [&]() -> unsigned {
    unsigned orr = 0;
#pragma unroll
    for (int u = 0; u < QT; ++u)
#pragma unroll
      for (int ks = 0; ks < 2; ++ks) {
        u32x4 w;
#pragma unroll
        for (int e = 0; e < 4; ++e) {
          const f32x4& sv = s[2 * ks + (e >> 1)][u];
          w[e] = cvtpk(__builtin_amdgcn_exp2f(sv[2 * (e & 1)]), __builtin_amdgcn_exp2f(sv[2 * (e & 1) + 1]));
        }
        orr |= w[0] | w[1];
        orr |= w[2] | w[3];
        pf[u][ks] = __builtin_bit_cast(bf16x8, w);
      }
    return orr;
  };
  readK(0);
  unsigned fl = 3u;
#pragma clang loop unroll(disable)
  for (int t = 0; t < 8; ++t) {
    SBAR();
    readK(1);
    qk(0);
#pragma unroll
    for (int k = 0; k < 8; ++k) {
      __builtin_amdgcn_sched_group_barrier(0x100, 1, 0);
      __builtin_amdgcn_sched_group_barrier(0x008, QT, 0);
    }
    SBAR();
    if (rfl(fl) < (unsigned)(2 * t + 2)) lds_wait_ge(aLAND, (unsigned)(2 * t + 2));
    SBAR();
    readV(0);
    fl = *reinterpret_cast<volatile LAS unsigned*>(aLAND);
    qk(1);
#pragma unroll
    for (int k = 0; k < 8; ++k) {
      __builtin_amdgcn_sched_group_barrier(0x008, QT, 0);
      __builtin_amdgcn_sched_group_barrier(0x100, 2, 0);
    }
    __builtin_amdgcn_sched_group_barrier(0x100, 1, 0);
    SBAR();
    lds_pub(myp, (unsigned)(2 * t + 1));
    refill(2 * t);
    SBAR();
    bool slow = (t == 0);
    if (!slow) slow = __any((makeP() & 0x40004000u) != 0u);
    if (slow) {
#pragma unroll
      for (int u = 0; u < QT; ++u) {
        float a = max3f(s[0][u][0], s[0][u][1], s[0][u][2]), b2 = max3f(s[0][u][3], s[1][u][0], s[1][u][1]);
        a = max3f(a, s[1][u][2], s[1][u][3]); b2 = max3f(b2, s[2][u][0], s[2][u][1]);
        a = max3f(a, s[2][u][2], s[2][u][3]); b2 = max3f(b2, s[3][u][0], s[3][u][1]);
        a = max3f(a, s[3][u][2], s[3][u][3]);
        float dl = xmax_g(fmaxf(a, b2)) + OFF;
        if (t != 0) dl = fmaxf(dl, 0.f);
        const float alpha = __builtin_amdgcn_exp2f(-dl);
#pragma unroll
        for (int kt = 0; kt < 4; ++kt) s[kt][u] -= dl;
        negm[u] -= dl;
        if (t != 0) {
          lacc[u] *= alpha;
#pragma unroll
          for (int d = 0; d < 8; ++d) o[d][u] *= alpha;
        }
      }
      (void)makeP();
    }
    SBAR();
    readV(1);
    pv(0);
#pragma unroll
    for (int k = 0; k < 8; ++k) {
      __builtin_amdgcn_sched_group_barrier(0x100, 2, 0);
      __builtin_amdgcn_sched_group_barrier(0x008, QT, 0);
    }
    __builtin_amdgcn_sched_group_barrier(0x008, QT, 0);
    SBAR();
    {
      const unsigned need = (unsigned)(t < 7 ? 2 * t + 3 : 16);
      if (rfl(fl) < need) lds_wait_ge(aLAND, need);
    }
    SBAR();
    readK(0);
    fl = *reinterpret_cast<volatile LAS unsigned*>(aLAND);
    pv(1);
#pragma unroll
    for (int k = 0; k < 9; ++k) {
      __builtin_amdgcn_sched_group_barrier(0x008, QT, 0);
      __builtin_amdgcn_sched_group_barrier(0x100, 1, 0);
    }
    SBAR();
    lds_pub(myp, (unsigned)(2 * t + 2));
    refill(2 * t + 1);
  }
#pragma unroll
  for (int u = 0; u < QT; ++u) {
    const float inv = 1.0f / lacc[u][0];
#pragma unroll
    for (int s2 = 0; s2 < 4; ++s2)
      *reinterpret_cast<u32x4*>(op + (size_t)u * 131072 + s2 * 1024) = pack_pair(o[2 * s2][u] * inv, o[2 * s2 + 1][u] * inv);
  }
}

DI void q_stage0(const K1Ctx& c) {
  const int lane = c.lane, wave = c.wave;
  const int g = lane >> 4, qq = (lane >> 2) & 3, p = lane & 3;
  const unsigned r8 = (lane >> 2) & 7;
  const __amdgpu_buffer_rsrc_t rX = __builtin_amdgcn_make_buffer_rsrc((void*)c.xg, 0, SQ * DM * 2, 0x00020000);
  const __amdgpu_buffer_rsrc_t rW = __builtin_amdgcn_make_buffer_rsrc((void*)c.wg, 0, (unsigned)((size_t)DM * HD * 2 - (size_t)c.hp * 512), 0x00020000);
  const unsigned vw = (qq * 8192 + g * 64 + p * 16) ^ ((wave & 1) ? 32u : 0u);
  const unsigned vx = (r8 * 2048 + (lane >> 5) * 64 + p * 16) ^ ((wave & 1) ? 32u : 0u);
  const unsigned so = (unsigned)(64 * c.rot + 8 * wave) * 8192u, ld = c.lds0 + (unsigned)wave * 4096u, sx = (unsigned)c.rot * 128u;
  dma16d(rW, vw, so, ld); dma16d(rW, vw, so + 256u, ld + 1024u);
  dma16d(rW, vw, so + 4u * 8192u, ld + 2048u); dma16d(rW, vw, so + 4u * 8192u + 256u, ld + 3072u);
  dma16d(rX, vx, sx + (unsigned)wave * 16384u, c.lds0 + 32768u + (unsigned)wave * 1024u);
  if (wave < 6) dma16d(rX, vx, sx + (unsigned)(wave + 8) * 16384u, c.lds0 + 32768u + (unsigned)(wave + 8) * 1024u);
}

template <int QT>
DI void phase_q(const K1Ctx& c, f32x4 (&acc)[8][QT]) {
  const int lane = c.lane, wave = c.wave, hh = c.hh, w3 = c.w3;
  const unsigned lds0 = c.lds0;
  char* ows = c.ows; (void)ows;
  const int i = lane & 15, g = lane >> 4, qq = (lane >> 2) & 3, p = lane & 3;
  const unsigned wb = lds0 + hh * 1024 + g * 4096 + qq * 64 + p * 8;
  const unsigned wE0 = wb + ((g & 1) * 32), wO0 = wb + (((g & 1) ^ 1) * 32);
  const unsigned xa0 = lds0 + 32768 + w3 * 4096 + (i >> 3) * 1024 + (i & 7) * 64 + ((g ^ ((i >> 3) << 1)) * 16);
  const unsigned aLD = lds0 + L_FLAG + 64, aRD = lds0 + L_FLAG + 96;
  const unsigned gLD = aLD + 4u * (unsigned)(lane & 1);
  const unsigned aPR = aRD + 4u * (unsigned)(wave ^ 4);
#pragma unroll
  for (int t = 0; t < 8; ++t)
#pragma unroll
    for (int u = 0; u < QT; ++u) acc[t][u] = f32x4{0.f, 0.f, 0.f, 0.f};
  bf16x8 a0[8], b0[QT], a1[8], b1[QT];
  unsigned tj = 0; (void)tj;
  q_stage0(c);
  WAIT_VM(0);
  BAR();
  STAMP(2);
  q_read<QT>(a0, b0, wE0, wO0, xa0);
  __builtin_amdgcn_s_waitcnt(0xC07F);
  SBAR();
  q_read<QT>(a1, b1, wE0 + 16384, wO0 + 16384, xa0 + 512);
  q_mma<QT>(acc, a0, b0);
  q_sched<QT>();
  SBAR();
  __builtin_amdgcn_s_waitcnt(0xC07F);
  lds_pub(aRD + 4u * (unsigned)wave, 1u);
  lds_wait_ge(gLD, 2u);
  __builtin_amdgcn_s_waitcnt(0xC07F);
  unsigned ds = DSZ;
  for (int j = 1; j < 16; ++j) {
    SBAR();
    q_read<QT>(a0, b0, wE0 + ds, wO0 + ds, xa0 + ds);
    q_mma<QT>(acc, a1, b1);
    q_sched<QT>();
    SBAR();
    __builtin_amdgcn_s_waitcnt(0xC07F);
    SBAR();
    q_read<QT>(a1, b1, wE0 + ds + 16384, wO0 + ds + 16384, xa0 + ds + 512);
    const unsigned fld = *reinterpret_cast<volatile LAS unsigned*>(gLD);
    unsigned fpr = 0;
    if (QT == 2 && K1_DYNP) fpr = *reinterpret_cast<volatile LAS unsigned*>(aPR);
    q_mma<QT>(acc, a0, b0);
    q_sched<QT>();
    SBAR();
    __builtin_amdgcn_s_waitcnt(0xC07F);
    lds_pub(aRD + 4u * (unsigned)wave, (unsigned)(j + 1));
    if (QT == 2 && K1_DYNP) {
      if (rfl(fpr) >= (unsigned)(j + 1)) __builtin_amdgcn_s_setprio(1); else __builtin_amdgcn_s_setprio(0);
    }
    const unsigned need = (unsigned)(j < 15 ? j + 2 : 16);
    if (!__all(fld >= need)) {
      lds_wait_ge(gLD, need);
      if (QT == 2 && K1_DYNP == 2) __builtin_amdgcn_s_setprio(1);
    }
    ds = (ds == 2 * DSZ) ? 0u : ds + DSZ;
  }
  q_mma<QT>(acc, a1, b1);
  __builtin_amdgcn_s_setprio(0);
}

DI void phase_q_loader(const K1Ctx& c, unsigned vk0, unsigned vv) {
  const int lane = c.lane, hh = c.hh;
  const unsigned lds0 = c.lds0;
  char* ows = c.ows; (void)ows;
  const int g = lane >> 4, qq = (lane >> 2) & 3, p = lane & 3;
  const unsigned r8 = (lane >> 2) & 7;
  const unsigned aLD = lds0 + L_FLAG + 64 + 4u * (unsigned)hh, aRD = lds0 + L_FLAG + 96;
  const int rl = lane & 7;
  const unsigned gRD = aRD + 4u * (unsigned)(rl == 3 ? 0 : (rl == 6 ? 7 : rl));
  const __amdgpu_buffer_rsrc_t rX = __builtin_amdgcn_make_buffer_rsrc((void*)c.xg, 0, SQ * DM * 2, 0x00020000);
  const __amdgpu_buffer_rsrc_t rW = __builtin_amdgcn_make_buffer_rsrc((void*)(c.wg - 4096), 0, (unsigned)((size_t)DM * HD * 2 - (size_t)c.hp * 512) + 4096u, 0x00020000);
  const __amdgpu_buffer_rsrc_t rK = __builtin_amdgcn_make_buffer_rsrc((void*)c.kg, 0, SK * DH * 2, 0x00020000);
  const __amdgpu_buffer_rsrc_t rV = __builtin_amdgcn_make_buffer_rsrc((void*)c.vg, 0, SK * DH * 2, 0x00020000);
  const unsigned vwa = qq * 8192 + g * 64 + p * 16 + 4096u, vwb = (qq * 8192 + g * 64 + (p ^ 2) * 16) + 4096u;
  const unsigned vxa = (r8 * 2048 + (lane >> 5) * 64 + p * 16) ^ (hh ? 32u : 0u), vxb = vxa ^ 32u;
  const unsigned vk1 = vk0 ^ 32u;
  constexpr unsigned D1 = 256u - 1024u, D2 = 4u * 8192u - 2048u, D3 = 4u * 8192u + 256u - 3072u, DX = 16384u - 1024u;
  const unsigned wa1 = vwa + D1, wa2 = vwa + D2, wa3 = vwa + D3, wb1 = vwb + D1, wb2 = vwb + D2, wb3 = vwb + D3;
  const unsigned x1 = vxb + DX, x2 = vxa + 2 * DX, x3 = vxb + 3 * DX;
  auto issueA = [&](int j, unsigned ds) {
    j = (j & ~(K1_WIN - 1)) | ((j + c.rot) & (K1_WIN - 1));
    const unsigned so = (unsigned)(64 * j + 32 * hh) * 8192u, ld = lds0 + ds + (unsigned)hh * 16384u;
    dma16x4(rW, vwa, wa1, wa2, wa3, so, ld);
    dma16x4(rW, vwb, wb1, wb2, wb3, so + 8u * 8192u, ld + 4096u);
    dma16x4(rW, vwa, wa1, wa2, wa3, so + 16u * 8192u, ld + 8192u);
  };
  auto issueB = [&](int j, unsigned ds) {
    j = (j & ~(K1_WIN - 1)) | ((j + c.rot) & (K1_WIN - 1));
    const unsigned so = (unsigned)(64 * j + 32 * hh) * 8192u, ld = lds0 + ds + (unsigned)hh * 16384u;
    dma16x4(rW, vwb, wb1, wb2, wb3, so + 24u * 8192u, ld + 12288u);
    const unsigned sx = (unsigned)j * 128u + (unsigned)(7 * hh) * 16384u, lx = lds0 + ds + 32768u + (unsigned)(7 * hh) * 1024u;
    dma16x4(rX, vxa, x1, x2, x3, sx, lx);
    dma16x3(rX, vxa, x1, x2, sx + 4u * 16384u, lx + 4096u);
  };
  const int bd = c.bd;
  u32x4 kv[48];
  __builtin_amdgcn_s_setprio(K1_LPRIO);
  q_stage0(c);
  if (hh == 0 && lane < 32) reinterpret_cast<volatile LAS unsigned*>(lds0 + L_FLAG)[lane] = 0u;
  WAIT_VM(0);
  BAR();
  const __amdgpu_buffer_rsrc_t rS = __builtin_amdgcn_make_buffer_rsrc((void*)SEQ, 0, 256, 0x00020000);
  issueA(1, DSZ); issueB(1, DSZ);
  dma_flag(rS, 2u, aLD);
  unsigned dn = 2 * DSZ;
  int j = 0;
  unsigned ta = 0, tb = 0, tc = 0; (void)ta; (void)tb; (void)tc;
#define LT(v) do {} while (0)
  auto run = [&](int jend) {
#pragma clang loop unroll(disable)
    for (; j <= jend; ++j) {
      LT(ta);
      if (j >= 1) lds_wait_ge(gRD, (unsigned)j);
      LT(tb);
      issueA(j + 2, dn);
      issueB(j + 2, dn);
      dma_flag(rS, (unsigned)(j + 3), aLD);
      LT(tc);
      dn = (dn == 2 * DSZ) ? 0u : dn + DSZ;
    }
  };
  run(K1_JB0 + bd);
#pragma unroll
  for (int k = 0; k < 16; ++k) kv[k] = __builtin_amdgcn_raw_buffer_load_b128(rK, (k & 2) ? vk1 : vk0, (k >> 1) * 2048 + (k & 1) * 128, KVAUX);
  run(K1_JB1 + bd);
#pragma unroll
  for (int k = 0; k < 16; ++k) kv[16 + k] = __builtin_amdgcn_raw_buffer_load_b128(rV, vv, (k >> 1) * 2048 + (k & 1) * 128, KVAUX);
  run(K1_JB2 + bd);
#pragma unroll
  for (int k = 0; k < 16; ++k) kv[32 + k] = __builtin_amdgcn_raw_buffer_load_b128(rK, (k & 2) ? vk1 : vk0, 16384 + (k >> 1) * 2048 + (k & 1) * 128, KVAUX);
  run(13);
  lds_wait_ge(gRD, 14u);
  {
    const unsigned a = lds0 + ring_slot(hh, 0) + lane * 16;
#pragma unroll
    for (int k = 0; k < 16; ++k) *reinterpret_cast<LAS u32x4*>(a + k * 1024) = kv[k];
  }
  lds_wait_ge(gRD, 15u);
  {
    const unsigned a1 = lds0 + ring_slot(hh, 1) + lane * 16, a2 = lds0 + ring_slot(hh, 2) + lane * 16;
#pragma unroll
    for (int k = 0; k < 16; ++k) *reinterpret_cast<LAS u32x4*>(a1 + k * 1024) = kv[16 + k];
#pragma unroll
    for (int k = 0; k < 16; ++k) *reinterpret_cast<LAS u32x4*>(a2 + k * 1024) = kv[32 + k];
  }
  __builtin_amdgcn_s_setprio(0);
}

__global__ __launch_bounds__(512, 2) __attribute__((amdgpu_waves_per_eu(2, 2))) void k1_qattn(
    const __hip_bfloat16* __restrict__ xA, const __hip_bfloat16* __restrict__ kA, const __hip_bfloat16* __restrict__ vA,
    const __hip_bfloat16* __restrict__ wqA, const __hip_bfloat16* __restrict__ xB, const __hip_bfloat16* __restrict__ kB,
    const __hip_bfloat16* __restrict__ vB, const __hip_bfloat16* __restrict__ wqB, __hip_bfloat16* __restrict__ ows) {
  extern __shared__ __attribute__((aligned(16))) char shm[];
  const unsigned lds0 = (unsigned)(uintptr_t)(LAS char*)shm;
  const int tid = threadIdx.x, lane = tid & 63;
  const int wave = (int)rfl((unsigned)tid >> 6);
  const int hh = wave >> 2, w3 = (wave & 3) ^ ((wave >= 6) ? 1 : 0);
  const int bid = blockIdx.x, xcd = bid & 7, jj = bid >> 3;
  const int side = xcd >> 2, b = jj & 7, hpl = jj >> 3, hp = (xcd & 3) * 4 + hpl;
  K1Ctx c;
  c.xg = (const char*)(side ? xB : xA) + (size_t)b * SQ * DM * 2;
  c.wg = (const char*)(side ? wqB : wqA) + (size_t)hp * 512;
  c.kg = (const char*)(side ? kB : kA) + (size_t)(b * NH + 2 * hp + hh) * SK * DH * 2;
  c.vg = (const char*)(side ? vB : vA) + (size_t)(b * NH + 2 * hp + hh) * SK * DH * 2;
  c.lds0 = lds0; c.wave = wave; c.hh = hh; c.w3 = w3; c.lane = lane; c.b = b; c.hpl = hpl; c.hp = hp; c.jj = jj; c.ows = (char*)ows;
  c.bd = ((jj >> 2) & 1) * K1_JBD;
  c.rot = (K1_WIN == 4) ? ((b + hpl) & 3) : (K1_WIN == 8) ? ((b + 2 * hpl) & 7) : (K1_WIN == 2) ? ((b + hpl) & 1) : 0;
  if (tid == 0) *reinterpret_cast<unsigned*>((char*)ows + WS_FLAG + (size_t)bid * 128) = 0u;
  const unsigned r8 = (lane >> 2) & 7;
  const unsigned vk0 = r8 * 256 + (lane >> 5) * 64 + (lane & 3) * 16;
  const unsigned vv = r8 * 256 + (lane >> 5) * 64 + ((lane & 3) ^ ((r8 >> 2) << 1)) * 16;
  char* op = (char*)ows + ((size_t)((side * NB + b) * 7 + 2 * w3) * 128 + (2 * hp + hh) * 4) * 1024 + lane * 16;
  const unsigned qx = lds0 + L_QX + (unsigned)hh * 4096u + lane * 16;
  STAMP(0); STAMPC(8);
  if (w3 < 3) {
    bf16x8 qf[2][4];
    if (w3 < 2) {
      f32x4 acc[8][2];
      phase_q<2>(c, acc);
#pragma unroll
      for (int s = 0; s < 4; ++s)
#pragma unroll
        for (int u = 0; u < 2; ++u) qf[u][s] = __builtin_bit_cast(bf16x8, pack_pair(acc[2 * s][u] * SM_SC, acc[2 * s + 1][u] * SM_SC));
    } else {
      f32x4 acc[8][3];
      phase_q<3>(c, acc);
#pragma unroll
      for (int s = 0; s < 4; ++s) {
#pragma unroll
        for (int u = 0; u < 2; ++u) qf[u][s] = __builtin_bit_cast(bf16x8, pack_pair(acc[2 * s][u] * SM_SC, acc[2 * s + 1][u] * SM_SC));
        *reinterpret_cast<LAS u32x4*>(qx + s * 1024) = pack_pair(acc[2 * s][2] * SM_SC, acc[2 * s + 1][2] * SM_SC);
      }
    }
    BAR();
    STAMP(1); STAMPC(9);
    if (hh == 0) phase_a<2, 5>(c, qf, op, vk0, vv);
    else phase_a<2, 4>(c, qf, op, vk0, vv);
  } else {
    phase_q_loader(c, vk0, vv);
    BAR();
    bf16x8 qf[1][4];
#pragma unroll
    for (int s = 0; s < 4; ++s) qf[0][s] = lds_b128(qx + s * 1024);
    if (hh == 0) phase_a<1, 5>(c, qf, op, vk0, vv);
    else phase_a<1, 4>(c, qf, op, vk0, vv);
  }
  STAMP(6); STAMPC(11);
}

constexpr int NS2 = 9;
constexpr unsigned L2_FLAG = NS2 * SLOT;
constexpr unsigned K2_LDS = NS2 * SLOT + 1024;
#define K2_WNT 1
typedef __attribute__((address_space(1))) unsigned gu32;

__global__ __launch_bounds__(512, 2) __attribute__((amdgpu_waves_per_eu(2, 2))) void k2_oproj(
    __hip_bfloat16* __restrict__ ows, const __hip_bfloat16* __restrict__ woA, const __hip_bfloat16* __restrict__ woB,
    __hip_bfloat16* __restrict__ out) {
  extern __shared__ __attribute__((aligned(16))) char shm[];
  const unsigned lds0 = (unsigned)(uintptr_t)(LAS char*)shm;
  const int tid = threadIdx.x, lane = tid & 63;
  const int wave = (int)rfl((unsigned)tid >> 6);
  const int bid = blockIdx.x, xcd = bid & 7, jj = bid >> 3;
  const int side = xcd >> 2, kq = xcd & 3, bp = jj & 3, nt = jj >> 2;
  const int i = lane & 15, g = lane >> 4, qq = (lane >> 2) & 3, p = lane & 3;
  const unsigned aFULL = lds0 + L2_FLAG, aPROG = lds0 + L2_FLAG + 64;
  const int tile = (side * 4 + bp) * 8 + nt;
  gu32* flags = (gu32*)((char*)ows + WS_FLAG + (size_t)tile * 512);
  STAMP2(0);
  f32x4 acc[8][2];
  if (wave == 7) {
    __builtin_amdgcn_s_setprio(3);
    if (lane < 32) reinterpret_cast<volatile LAS unsigned*>(lds0 + L2_FLAG)[lane] = 0u;
    const char* wg = (const char*)(side ? woB : woA) + (size_t)kq * 1024 * 2048 + (size_t)nt * 256;
    const __amdgpu_buffer_rsrc_t rW = __builtin_amdgcn_make_buffer_rsrc((void*)wg, 0, (unsigned)(1024 * 2048 - nt * 256), 0x00020000);
    const unsigned vw0 = qq * 2048 + g * 64 + p * 16, vw1 = vw0 ^ 32u;
    auto issue = [&](int n, unsigned slot) {
#pragma unroll
      for (int pc = 0; pc < 16; ++pc) {
        if (K2_WNT) dma16nt(rW, ((pc >> 1) & 1) ? vw1 : vw0, ((unsigned)n * 64u + (unsigned)pc * 4u) * 2048u, lds0 + slot + pc * 1024u);
        else dma16(rW, ((pc >> 1) & 1) ? vw1 : vw0, ((unsigned)n * 64u + (unsigned)pc * 4u) * 2048u, lds0 + slot + pc * 1024u);
      }
    };
    issue(0, 0);
    BAR();
    issue(1, SLOT); issue(2, 2 * SLOT);
    unsigned slot = 3 * SLOT;
    for (int n = 3; n < 16; ++n) {
      WAIT_VM(32);
      lds_pub(aFULL, (unsigned)(n - 2));
      if (n >= NS2) lds_wait_ge(aPROG + 4u * (unsigned)(lane < 7 ? lane : 6), (unsigned)(n - NS2 + 1));
      issue(n, slot);
      slot = (slot == (NS2 - 1) * SLOT) ? 0u : slot + SLOT;
    }
    WAIT_VM(32);
    lds_pub(aFULL, 14u);
    WAIT_VM(16);
    lds_pub(aFULL, 15u);
    WAIT_VM(0);
    lds_pub(aFULL, 16u);
  } else {
    const char* og = (const char*)ows + ((size_t)((side * NB + 2 * bp) * 7 + wave) * 128 + kq * 32) * 1024;
    const __amdgpu_buffer_rsrc_t rO0 = __builtin_amdgcn_make_buffer_rsrc((void*)og, 0, 32768, 0x00020000);
    const __amdgpu_buffer_rsrc_t rO1 = __builtin_amdgcn_make_buffer_rsrc((void*)(og + 7 * 131072), 0, 32768, 0x00020000);
    const unsigned wb = lds0 + g * 2048 + qq * 64 + p * 8;
    const unsigned wE = wb + (g & 1) * 32, wO = wb + ((g & 1) ^ 1) * 32;
    u32x4 of[8][2];
#pragma unroll
    for (int k = 0; k < 8; ++k) {
      of[k][0] = __builtin_amdgcn_raw_buffer_load_b128(rO0, lane * 16, k * 1024, 0);
      of[k][1] = __builtin_amdgcn_raw_buffer_load_b128(rO1, lane * 16, k * 1024, 0);
    }
#pragma unroll
    for (int t = 0; t < 8; ++t) { acc[t][0] = f32x4{0.f, 0.f, 0.f, 0.f}; acc[t][1] = f32x4{0.f, 0.f, 0.f, 0.f}; }
    bf16x8 af[2][8];
    auto readA = [&](bf16x8 (&a)[8], unsigned sl) {
      unsigned aE = wE + sl, aO = wO + sl;
      asm volatile("" : "+v"(aE), "+v"(aO));
#pragma unroll
      for (int t = 0; t < 8; ++t) {
        const unsigned ad = ((t & 1) ? aO : aE) + (t >> 1) * 256;
        a[t] = cat8(lds_tr(ad), lds_tr(ad + 1024));
      }
    };
    BAR();
    lds_wait_ge(aFULL, 1u);
    readA(af[0], 0u);
    unsigned slot = 0;
    for (int so = 0; so < 16; so += 4) {
#pragma unroll
      for (int rr = 0; rr < 4; ++rr) {
        const int st = so + rr;
        const unsigned nslot = (slot == (NS2 - 1) * SLOT) ? 0u : slot + SLOT;
        const unsigned need = (unsigned)(st + 2 > 16 ? 16 : st + 2);
        unsigned fv = 0;
#pragma unroll
        for (int hs = 0; hs < 2; ++hs) {
          __builtin_amdgcn_s_waitcnt(0xC07F);
          if (hs == 1) lds_pub(aPROG + 4u * (unsigned)wave, (unsigned)(st + 1));
          SBAR();
          if (hs == 0) fv = *reinterpret_cast<volatile LAS unsigned*>(aFULL);
          readA(af[hs ^ 1], hs == 0 ? slot + 8192u : nslot);
          const int k = rr * 2 + hs;
#pragma unroll
          for (int t = 0; t < 8; ++t) {
            acc[t][0] = mfma16(af[hs][t], __builtin_bit_cast(bf16x8, of[k][0]), acc[t][0]);
            acc[t][1] = mfma16(af[hs][t], __builtin_bit_cast(bf16x8, of[k][1]), acc[t][1]);
          }
          of[k][0] = __builtin_amdgcn_raw_buffer_load_b128(rO0, lane * 16, ((st + 4) * 2 + hs) * 1024, 0);
          of[k][1] = __builtin_amdgcn_raw_buffer_load_b128(rO1, lane * 16, ((st + 4) * 2 + hs) * 1024, 0);
          if (hs == 0) __builtin_amdgcn_sched_group_barrier(0x100, 1, 0);
#pragma unroll
          for (int k2 = 0; k2 < 8; ++k2) {
            __builtin_amdgcn_sched_group_barrier(0x008, 1, 0);
            __builtin_amdgcn_sched_group_barrier(0x100, 2, 0);
          }
          __builtin_amdgcn_sched_group_barrier(0x008, 8, 0);
          __builtin_amdgcn_sched_group_barrier(0x020, 2, 0);
          SBAR();
          if (hs == 0 && rfl(fv) < need) lds_wait_ge(aFULL, need);
        }
        slot = nslot;
      }
    }
    STAMP2(1);
    const char* pb = (const char*)ows + WS_PART + (size_t)tile * (16 * 7 * 4096);
    const __amdgpu_buffer_rsrc_t rP = __builtin_amdgcn_make_buffer_rsrc((void*)pb, 0, 16 * 7 * 4096, 0x00020000);
#pragma unroll
    for (int t = 0; t < 4; ++t) {
      if (t != kq) {
        const unsigned off = (unsigned)(((t * 4 + kq) * 7 + wave) * 4096);
#pragma unroll
        for (int e = 0; e < 4; ++e)
          __builtin_amdgcn_raw_buffer_store_b128(__builtin_bit_cast(u32x4, acc[2 * t + (e >> 1)][e & 1]), rP, lane * 16, off + e * 1024, 16);
      }
    }
  }
  WAIT_VM(0);
  BAR();
  if (wave == 7) {
    if (lane == 0) __hip_atomic_store(flags + kq * 32, 1u, __ATOMIC_RELAXED, __HIP_MEMORY_SCOPE_AGENT);
    for (int it = 0; it < (1 << 20); ++it) {
      const unsigned v = __hip_atomic_load(flags + (lane & 3) * 32, __ATOMIC_RELAXED, __HIP_MEMORY_SCOPE_AGENT);
      if (__all(v != 0u)) break;
      __builtin_amdgcn_s_sleep(2);
    }
    asm volatile("" ::: "memory");
  }
  BAR();
  STAMP2(3);
  if (wave < 7) {
    const char* pb = (const char*)ows + WS_PART + (size_t)tile * (16 * 7 * 4096);
    const __amdgpu_buffer_rsrc_t rP = __builtin_amdgcn_make_buffer_rsrc((void*)pb, 0, 16 * 7 * 4096, 0x00020000);
    f32x4 sum[4];
#pragma unroll
    for (int e = 0; e < 4; ++e)
      sum[e] = (kq == 0) ? acc[0 + (e >> 1)][e & 1] : (kq == 1) ? acc[2 + (e >> 1)][e & 1] : (kq == 2) ? acc[4 + (e >> 1)][e & 1] : acc[6 + (e >> 1)][e & 1];
    u32x4 pv[3][4];
#pragma unroll
    for (int k = 0; k < 3; ++k) {
      const int src = (kq + 1 + k) & 3;
      const unsigned off = (unsigned)(((kq * 4 + src) * 7 + wave) * 4096);
#pragma unroll
      for (int e = 0; e < 4; ++e) pv[k][e] = __builtin_amdgcn_raw_buffer_load_b128(rP, lane * 16, off + e * 1024, 16);
    }
#pragma unroll
    for (int k = 0; k < 3; ++k)
#pragma unroll
      for (int e = 0; e < 4; ++e) sum[e] += __builtin_bit_cast(f32x4, pv[k][e]);
    char* op = (char*)out + ((size_t)(2 * bp * SQ + 16 * wave + i) * (2 * DM) + side * DM + nt * 128 + kq * 32 + 8 * g) * 2;
    *reinterpret_cast<u32x4*>(op) = pack_pair(sum[0], sum[2]);
    *reinterpret_cast<u32x4*>(op + (size_t)SQ * (2 * DM) * 2) = pack_pair(sum[1], sum[3]);
  }
  STAMP2(2);
}

extern "C" void kernel_launch(void* const* d_in, const int* in_sizes, int n_in, void* d_out, int out_size, void* d_ws, size_t ws_size,
                              hipStream_t stream) {
  (void)in_sizes; (void)n_in; (void)out_size; (void)ws_size;
  typedef const __hip_bfloat16* cp;
  (void)hipFuncSetAttribute((const void*)k1_qattn, hipFuncAttributeMaxDynamicSharedMemorySize, (int)K1_LDS);
  (void)hipFuncSetAttribute((const void*)k2_oproj, hipFuncAttributeMaxDynamicSharedMemorySize, (int)K2_LDS);
  __hip_bfloat16* ows = (__hip_bfloat16*)d_ws;
  k1_qattn<<<256, 512, K1_LDS, stream>>>((cp)d_in[0], (cp)d_in[1], (cp)d_in[2], (cp)d_in[3], (cp)d_in[5], (cp)d_in[6], (cp)d_in[7],
                                         (cp)d_in[8], ows);
  k2_oproj<<<256, 512, K2_LDS, stream>>>(ows, (cp)d_in[4], (cp)d_in[9], (__hip_bfloat16*)d_out);
}
```

```cpp
#include <hip/hip_runtime.h>
#include <hip/hip_bf16.h>
#include <stdint.h>

typedef __bf16 bf16x2 __attribute__((ext_vector_type(2)));
typedef __bf16 bf16x4 __attribute__((ext_vector_type(4)));
typedef __bf16 bf16x8 __attribute__((ext_vector_type(8)));
typedef float f32x2 __attribute__((ext_vector_type(2)));
typedef float f32x4 __attribute__((ext_vector_type(4)));
typedef unsigned u32x4 __attribute__((ext_vector_type(4)));
#define LAS __attribute__((address_space(3)))
#define DI __device__ __forceinline__

constexpr size_t WS_PART = (size_t)16 << 20, WS_FLAG = (size_t)48 << 20;
constexpr int NB = 8, SQ = 112, DM = 1024, NH = 32, SK = 512, DH = 128, HD = NH * DH;

#define K1_ROT 1
#define K1_PF0 2
#define K1_DEPTH 3

DI unsigned rfl(unsigned x) { return __builtin_amdgcn_readfirstlane(x); }
DI void dma16(__amdgpu_buffer_rsrc_t rsrc, unsigned voff, unsigned soff, unsigned lds) {
  asm volatile("s_mov_b32 m0, %0\n\ts_nop 0\n\tbuffer_load_dwordx4 %1, %2, %3 offen lds"
               :: "s"(lds), "v"(voff), "s"(rsrc), "s"(soff) : "m0", "memory");
}
DI void dma16nt(__amdgpu_buffer_rsrc_t rsrc, unsigned voff, unsigned soff, unsigned lds) {
  asm volatile("s_mov_b32 m0, %0\n\ts_nop 0\n\tbuffer_load_dwordx4 %1, %2, %3 offen nt lds"
               :: "s"(lds), "v"(voff), "s"(rsrc), "s"(soff) : "m0", "memory");
}
#define K1_DNT 0
#define K1_DYNP 2
#define K1_WIN 4
#define DPOL ""
DI void dma16d(__amdgpu_buffer_rsrc_t rsrc, unsigned voff, unsigned soff, unsigned lds) {
  asm volatile("s_mov_b32 m0, %0\n\ts_nop 0\n\tbuffer_load_dwordx4 %1, %2, %3 offen" DPOL " lds"
               :: "s"(lds), "v"(voff), "s"(rsrc), "s"(soff) : "m0", "memory");
}
DI void dma16x4(__amdgpu_buffer_rsrc_t rsrc, unsigned v0, unsigned v1, unsigned v2, unsigned v3, unsigned soff, unsigned lds) {
  asm volatile("s_mov_b32 m0, %0\n\ts_nop 0\n\t"
               "buffer_load_dwordx4 %1, %5, %6 offen" DPOL " lds\n\t"
               "buffer_load_dwordx4 %2, %5, %6 offen offset:1024" DPOL " lds\n\t"
               "buffer_load_dwordx4 %3, %5, %6 offen offset:2048" DPOL " lds\n\t"
               "buffer_load_dwordx4 %4, %5, %6 offen offset:3072" DPOL " lds"
               :: "s"(lds), "v"(v0), "v"(v1), "v"(v2), "v"(v3), "s"(rsrc), "s"(soff) : "m0", "memory");
}
DI void dma16x3(__amdgpu_buffer_rsrc_t rsrc, unsigned v0, unsigned v1, unsigned v2, unsigned soff, unsigned lds) {
  asm volatile("s_mov_b32 m0, %0\n\ts_nop 0\n\t"
               "buffer_load_dwordx4 %1, %4, %5 offen" DPOL " lds\n\t"
               "buffer_load_dwordx4 %2, %4, %5 offen offset:1024" DPOL " lds\n\t"
               "buffer_load_dwordx4 %3, %4, %5 offen offset:2048" DPOL " lds"
               :: "s"(lds), "v"(v0), "v"(v1), "v"(v2), "s"(rsrc), "s"(soff) : "m0", "memory");
}
__device__ const unsigned SEQ[64] = {0, 1, 2, 3, 4, 5, 6, 7, 8, 9, 10, 11, 12, 13, 14, 15, 16, 17, 18, 19, 20, 21, 22, 23, 24, 25, 26, 27, 28, 29, 30, 31,
                                     32, 33, 34, 35, 36, 37, 38, 39, 40, 41, 42, 43, 44, 45, 46, 47, 48, 49, 50, 51, 52, 53, 54, 55, 56, 57, 58, 59, 60, 61, 62, 63};
DI void dma_flag(__amdgpu_buffer_rsrc_t rseq, unsigned val, unsigned lds) {
  asm volatile("s_mov_b32 m0, %0\n\ts_mov_b64 exec, 1\n\tbuffer_load_dword off, %1, %2 lds\n\ts_mov_b64 exec, -1"
               :: "s"(lds), "s"(rseq), "s"(val * 4u) : "m0", "memory");
}
DI void touch4(__amdgpu_buffer_rsrc_t rsrc, unsigned voff, unsigned soff, unsigned lds) {
  asm volatile("s_mov_b32 m0, %0\n\ts_nop 0\n\tbuffer_load_dword %1, %2, %3 offen lds"
               :: "s"(lds), "v"(voff), "s"(rsrc), "s"(soff) : "m0", "memory");
}
DI void touch4nt(__amdgpu_buffer_rsrc_t rsrc, unsigned voff, unsigned soff, unsigned lds) {
  asm volatile("s_mov_b32 m0, %0\n\ts_nop 0\n\tbuffer_load_dword %1, %2, %3 offen nt lds"
               :: "s"(lds), "v"(voff), "s"(rsrc), "s"(soff) : "m0", "memory");
}
#define WAIT_VM(n) asm volatile("s_waitcnt vmcnt(" #n ")" ::: "memory")
#define BAR() do { asm volatile("" ::: "memory"); __builtin_amdgcn_s_waitcnt(0xC07F); asm volatile("s_barrier" ::: "memory"); } while (0)
#define SBAR() __builtin_amdgcn_sched_barrier(0)
DI void lds_pub(unsigned addr, unsigned val) {
  asm volatile("s_mov_b64 exec, 1\n\tds_write_b32 %0, %1\n\ts_mov_b64 exec, -1" :: "v"(addr), "v"(val) : "memory");
}
DI void lds_wait_ge(unsigned addr, unsigned need) {
  unsigned tmp, cnt = 1u << 22;
  asm volatile(
      "LW%=:\n\t"
      "ds_read_b32 %0, %2\n\t"
      "s_waitcnt lgkmcnt(0)\n\t"
      "v_cmp_le_u32 vcc, %3, %0\n\t"
      "s_sub_u32 %1, %1, 1\n\t"
      "s_cbranch_scc1 LE%=\n\t"
      "s_cmp_eq_u64 vcc, -1\n\t"
      "s_cbranch_scc1 LE%=\n\t"
      "s_sleep 1\n\t"
      "s_branch LW%=\n\t"
      "LE%=:"
      : "=&v"(tmp), "+s"(cnt) : "v"(addr), "s"(need) : "memory", "scc", "vcc");
}
DI bf16x8 lds_b128(unsigned a) { return *reinterpret_cast<LAS bf16x8*>(a); }
DI bf16x4 lds_tr(unsigned a) { return __builtin_amdgcn_ds_read_tr16_b64_v4bf16(reinterpret_cast<LAS bf16x4*>(a)); }
DI bf16x8 cat8(bf16x4 a, bf16x4 b) { return __builtin_shufflevector(a, b, 0, 1, 2, 3, 4, 5, 6, 7); }
DI unsigned cvtpk(float lo, float hi) {
  f32x2 v = {lo, hi};
  bf16x2 b = __builtin_convertvector(v, bf16x2);
  return __builtin_bit_cast(unsigned, b);
}
DI f32x4 mfma16(bf16x8 a, bf16x8 b, f32x4 c) { return __builtin_amdgcn_mfma_f32_16x16x32_bf16(a, b, c, 0, 0, 0); }
DI u32x4 pack_pair(f32x4 A, f32x4 B) {
  unsigned a0 = cvtpk(A[0], A[1]), a1 = cvtpk(A[2], A[3]), b0 = cvtpk(B[0], B[1]), b1 = cvtpk(B[2], B[3]);
  auto r0 = __builtin_amdgcn_permlane32_swap(a0, b0, false, false);
  auto s0 = __builtin_amdgcn_permlane16_swap(r0[0], r0[1], false, false);
  auto r1 = __builtin_amdgcn_permlane32_swap(a1, b1, false, false);
  auto s1 = __builtin_amdgcn_permlane16_swap(r1[0], r1[1], false, false);
  u32x4 o = {s0[0], s1[0], s0[1], s1[1]};
  return o;
}
DI float xmax_g(float v) {
  auto r = __builtin_amdgcn_permlane32_swap(__float_as_uint(v), __float_as_uint(v), false, false);
  v = fmaxf(__uint_as_float(r[0]), __uint_as_float(r[1]));
  auto s = __builtin_amdgcn_permlane16_swap(__float_as_uint(v), __float_as_uint(v), false, false);
  return fmaxf(__uint_as_float(s[0]), __uint_as_float(s[1]));
}
DI float xsum_g(float v) {
  auto r = __builtin_amdgcn_permlane32_swap(__float_as_uint(v), __float_as_uint(v), false, false);
  v = __uint_as_float(r[0]) + __uint_as_float(r[1]);
  auto s = __builtin_amdgcn_permlane16_swap(__float_as_uint(v), __float_as_uint(v), false, false);
  return __uint_as_float(s[0]) + __uint_as_float(s[1]);
}

#define STAMP(idx) do {} while (0)
#define STAMP2(idx) do {} while (0)
#define STAMPC(idx) do {} while (0)
#define TACC(acc, last) do {} while (0)
#define TPUT(idx, v) do {} while (0)
DI unsigned long long tnow() { return 0; }
constexpr unsigned SLOT = 16384;
constexpr unsigned DSZ = 47104;
constexpr unsigned L_FLAG = 9 * SLOT;
constexpr unsigned L_QX = L_FLAG + 1024;
constexpr unsigned K1_LDS = L_QX + 8192;
#define K1_TNT 1
#define K1_LPRIO 3
#define K1_JT1 0
#define K1_JT2 5
#define K1_JB0 3
#define K1_JB1 7
#define K1_JB2 11
#define K1_JBD 1

DI void gtouch(const char* p, unsigned lds) {
  if (K1_TNT) asm volatile("s_mov_b32 m0, %0\n\ts_nop 0\n\tglobal_load_lds_dword %1, off nt" :: "s"(lds), "v"(p) : "m0", "memory");
  else asm volatile("s_mov_b32 m0, %0\n\ts_nop 0\n\tglobal_load_lds_dword %1, off" :: "s"(lds), "v"(p) : "m0", "memory");
}
DI unsigned ring_slot(int hh, int r) { return (((hh ? 0x2874u : 0x10653u) >> (4 * r)) & 15u) * SLOT; }

template <int QT>
DI void q_read(bf16x8 (&a)[8], bf16x8 (&b)[QT], unsigned wE, unsigned wO, unsigned xa) {
  asm volatile("" : "+v"(wE), "+v"(wO), "+v"(xa));
#pragma unroll
  for (int u = 0; u < QT; ++u) b[u] = lds_b128(xa + u * 2048);
#pragma unroll
  for (int t = 0; t < 8; ++t) {
    const unsigned base = (t & 1) ? wO : wE;
    a[t] = cat8(lds_tr(base + (t >> 1) * 256), lds_tr(base + (t >> 1) * 256 + 2048));
  }
}
template <int QT>
DI void q_mma(f32x4 (&acc)[8][QT], const bf16x8 (&a)[8], const bf16x8 (&b)[QT]) {
#pragma unroll
  for (int t = 0; t < 8; ++t)
#pragma unroll
    for (int u = 0; u < QT; ++u) acc[t][u] = mfma16(a[t], b[u], acc[t][u]);
}
template <int QT>
DI void q_sched() {
#pragma unroll
  for (int k = 0; k < 10; ++k) {
    __builtin_amdgcn_sched_group_barrier(0x008, 1, 0);
    __builtin_amdgcn_sched_group_barrier(0x100, 2, 0);
  }
  __builtin_amdgcn_sched_group_barrier(0x008, 8 * QT - 10, 0);
}

constexpr float SM_SC = 0.08838834764831845f * 1.4426950408889634f;
struct K1Ctx {
  const char *xg, *wg, *kg, *vg;
  unsigned lds0;
  int wave, hh, w3, lane, b, hpl, hp, jj, bd, rot, krot;
  char* ows;
  __attribute__((address_space(1))) unsigned long long* oflag;
  __attribute__((address_space(1))) unsigned long long* epoch;
};
constexpr unsigned L_TAG = L_FLAG + 200;
DI unsigned long long get_tag(unsigned lds0) { return *reinterpret_cast<volatile LAS unsigned long long*>(lds0 + L_TAG); }

#define K1_KVNT 1
#define KVPOL " nt"
#define KVAUX 2
DI void dma16x4nt(__amdgpu_buffer_rsrc_t rsrc, unsigned v0, unsigned v1, unsigned v2, unsigned v3, unsigned soff, unsigned lds) {
  asm volatile("s_mov_b32 m0, %0\n\ts_nop 0\n\t"
               "buffer_load_dwordx4 %1, %5, %6 offen" KVPOL " lds\n\t"
               "buffer_load_dwordx4 %2, %5, %6 offen offset:1024" KVPOL " lds\n\t"
               "buffer_load_dwordx4 %3, %5, %6 offen offset:2048" KVPOL " lds\n\t"
               "buffer_load_dwordx4 %4, %5, %6 offen offset:3072" KVPOL " lds"
               :: "s"(lds), "v"(v0), "v"(v1), "v"(v2), "v"(v3), "s"(rsrc), "s"(soff) : "m0", "memory");
}
DI float max3f(float a, float b, float c) { return fmaxf(fmaxf(a, b), c); }
#define K1_OFF 16.0f

typedef __attribute__((address_space(1))) unsigned long long gu64;
template <int QT, int NS>
DI void phase_a(const K1Ctx& c, const bf16x8 (&qf)[QT][4], const char* op, unsigned vk0, unsigned vv) {
  const int lane = c.lane, w3 = c.w3, hh = c.hh;
  const unsigned lds0 = c.lds0;
  const int i = lane & 15, g = lane >> 4, qq = (lane >> 2) & 3, p = lane & 3;
  const unsigned aLAND = lds0 + L_FLAG + 16u * (unsigned)hh, aPROG = lds0 + L_FLAG + 32u + 16u * (unsigned)hh;
  const unsigned kb = lds0 + (i >> 3) * 2048 + (i & 7) * 64 + ((g ^ ((i >> 3) << 1)) * 16);
  const unsigned vb0 = lds0 + (g >> 1) * 2048 + (4 * (g & 1) + qq) * 64 + p * 8;
  const unsigned vbE = vb0 + (g & 1) * 32, vbO = vb0 + ((g & 1) ^ 1) * 32;
  const unsigned gp = aPROG + 4u * (unsigned)(lane & 3), myp = aPROG + 4u * (unsigned)w3;
  const __amdgpu_buffer_rsrc_t rK = __builtin_amdgcn_make_buffer_rsrc((void*)(c.kg - 1024), 0, SK * DH * 2 + 1024, 0x00020000);
  const __amdgpu_buffer_rsrc_t rV = __builtin_amdgcn_make_buffer_rsrc((void*)(c.vg - 1024), 0, SK * DH * 2 + 1024, 0x00020000);
  const __amdgpu_buffer_rsrc_t rS = __builtin_amdgcn_make_buffer_rsrc((void*)SEQ, 0, 256, 0x00020000);
  const unsigned k0 = vk0 + 1024u, k1 = vk0 + 128u, k2 = (vk0 ^ 32u) + 1024u, k3 = (vk0 ^ 32u) + 128u;
  const unsigned v0 = vv + 1024u, v1 = vv + 128u;
  auto issue = [&](int m, unsigned sl) {
    const unsigned so = (unsigned)(((m >> 1) + c.krot) & 7) * 16384u;
    const unsigned ld = lds0 + sl;
    if (m & 1) {
#pragma unroll
      for (int k = 0; k < 4; ++k) dma16x4nt(rV, v0, v1, v0, v1, so + k * 4096u, ld + k * 4096u);
    } else {
#pragma unroll
      for (int k = 0; k < 4; ++k) dma16x4nt(rK, k0, k1, k2, k3, so + k * 4096u, ld + k * 4096u);
    }
    dma_flag(rS, (unsigned)(m + 1), aLAND);
  };
  int rp = 0;
  unsigned slot = ring_slot(hh, 0);
  auto adv = [&]() { rp = (rp == NS - 1) ? 0 : rp + 1; slot = ring_slot(hh, rp); };
  int rq = 0;
  auto refill = [&](int m) {
    if (QT == 1) {
      if (m + NS < 16) {
        lds_wait_ge(gp, (unsigned)(m + 1));
        issue(m + NS, ring_slot(hh, rq));
      }
      rq = (rq == NS - 1) ? 0 : rq + 1;
    }
  };
  if (QT == 1) {
    lds_pub(aLAND, 3u);
    issue(3, ring_slot(hh, 3));
    if (NS == 5) issue(4, ring_slot(hh, 4));
  }
  bf16x8 kf[16], vf[16];
  auto readK = [&](int half) {
    unsigned ka = kb + slot;
    asm volatile("" : "+v"(ka));
#pragma unroll
    for (int kt = 2 * half; kt < 2 * half + 2; ++kt)
#pragma unroll
      for (int ks = 0; ks < 4; ++ks) kf[kt * 4 + ks] = lds_b128(ka + kt * 4096 + (ks >> 1) * 1024 + (ks & 1) * 512);
    if (half) adv();
  };
  auto readV = [&](int ks) {
    unsigned aE = vbE + slot, aO = vbO + slot;
    asm volatile("" : "+v"(aE), "+v"(aO));
#pragma unroll
    for (int d = 0; d < 8; ++d) {
      const unsigned base = ((d & 1) ? aO : aE) + (d >> 1) * 512;
      vf[d * 2 + ks] = cat8(lds_tr(base + (4 * ks) * 2048), lds_tr(base + (4 * ks + 2) * 2048));
    }
    if (ks) adv();
  };
  constexpr float OFF = K1_OFF;
  f32x4 o[8][QT], negm[QT], lacc[QT];
  const u32x4 onesu = {0x3f803f80u, 0x3f803f80u, 0x3f803f80u, 0x3f803f80u};
  const bf16x8 ones = __builtin_bit_cast(bf16x8, onesu);
#pragma unroll
  for (int u = 0; u < QT; ++u) {
    negm[u] = f32x4{-OFF, -OFF, -OFF, -OFF}; lacc[u] = f32x4{0.f, 0.f, 0.f, 0.f};
#pragma unroll
    for (int d = 0; d < 8; ++d) o[d][u] = f32x4{0.f, 0.f, 0.f, 0.f};
  }
  f32x4 s[4][QT];
  bf16x8 pf[QT][2];
  auto qk = [&](int half) {
#pragma unroll
    for (int kt = 2 * half; kt < 2 * half + 2; ++kt)
#pragma unroll
      for (int ks = 0; ks < 4; ++ks)
#pragma unroll
        for (int u = 0; u < QT; ++u) s[kt][u] = mfma16(kf[kt * 4 + ks], qf[u][ks], ks == 0 ? negm[u] : s[kt][u]);
  };
  auto pv = [&](int ks) {
#pragma unroll
    for (int d = 0; d < 8; ++d)
#pragma unroll
      for (int u = 0; u < QT; ++u) o[d][u] = mfma16(vf[d * 2 + ks], pf[u][ks], o[d][u]);
#pragma unroll
    for (int u = 0; u < QT; ++u) lacc[u] = mfma16(ones, pf[u][ks], lacc[u]);
  };
  auto makeP = [&]() -> unsigned {
    unsigned orr = 0;
#pragma unroll
    for (int u = 0; u < QT; ++u)
#pragma unroll
      for (int ks = 0; ks < 2; ++ks) {
        u32x4 w;
#pragma unroll
        for (int e = 0; e < 4; ++e) {
          const f32x4& sv = s[2 * ks + (e >> 1)][u];
          w[e] = cvtpk(__builtin_amdgcn_exp2f(sv[2 * (e & 1)]), __builtin_amdgcn_exp2f(sv[2 * (e & 1) + 1]));
        }
        orr |= w[0] | w[1];
        orr |= w[2] | w[3];
        pf[u][ks] = __builtin_bit_cast(bf16x8, w);
      }
    return orr;
  };
  readK(0);
  unsigned fl = 3u;
#pragma clang loop unroll(disable)
  for (int t = 0; t < 8; ++t) {
    SBAR();
    readK(1);
    qk(0);
#pragma unroll
    for (int k = 0; k < 8; ++k) {
      __builtin_amdgcn_sched_group_barrier(0x100, 1, 0);
      __builtin_amdgcn_sched_group_barrier(0x008, QT, 0);
    }
    SBAR();
    if (rfl(fl) < (unsigned)(2 * t + 2)) lds_wait_ge(aLAND, (unsigned)(2 * t + 2));
    SBAR();
    readV(0);
    fl = *reinterpret_cast<volatile LAS unsigned*>(aLAND);
    qk(1);
#pragma unroll
    for (int k = 0; k < 8; ++k) {
      __builtin_amdgcn_sched_group_barrier(0x008, QT, 0);
      __builtin_amdgcn_sched_group_barrier(0x100, 2, 0);
    }
    __builtin_amdgcn_sched_group_barrier(0x100, 1, 0);
    SBAR();
    lds_pub(myp, (unsigned)(2 * t + 1));
    refill(2 * t);
    SBAR();
    bool slow = (t == 0);
    if (!slow) slow = __any((makeP() & 0x40004000u) != 0u);
    if (slow) {
#pragma unroll
      for (int u = 0; u < QT; ++u) {
        float a = max3f(s[0][u][0], s[0][u][1], s[0][u][2]), b2 = max3f(s[0][u][3], s[1][u][0], s[1][u][1]);
        a = max3f(a, s[1][u][2], s[1][u][3]); b2 = max3f(b2, s[2][u][0], s[2][u][1]);
        a = max3f(a, s[2][u][2], s[2][u][3]); b2 = max3f(b2, s[3][u][0], s[3][u][1]);
        a = max3f(a, s[3][u][2], s[3][u][3]);
        float dl = xmax_g(fmaxf(a, b2)) + OFF;
        if (t != 0) dl = fmaxf(dl, 0.f);
        const float alpha = __builtin_amdgcn_exp2f(-dl);
#pragma unroll
        for (int kt = 0; kt < 4; ++kt) s[kt][u] -= dl;
        negm[u] -= dl;
        if (t != 0) {
          lacc[u] *= alpha;
#pragma unroll
          for (int d = 0; d < 8; ++d) o[d][u] *= alpha;
        }
      }
      (void)makeP();
    }
    SBAR();
    readV(1);
    pv(0);
#pragma unroll
    for (int k = 0; k < 8; ++k) {
      __builtin_amdgcn_sched_group_barrier(0x100, 2, 0);
      __builtin_amdgcn_sched_group_barrier(0x008, QT, 0);
    }
    __builtin_amdgcn_sched_group_barrier(0x008, QT, 0);
    SBAR();
    {
      const unsigned need = (unsigned)(t < 7 ? 2 * t + 3 : 16);
      if (rfl(fl) < need) lds_wait_ge(aLAND, need);
    }
    SBAR();
    readK(0);
    fl = *reinterpret_cast<volatile LAS unsigned*>(aLAND);
    pv(1);
#pragma unroll
    for (int k = 0; k < 9; ++k) {
      __builtin_amdgcn_sched_group_barrier(0x008, QT, 0);
      __builtin_amdgcn_sched_group_barrier(0x100, 1, 0);
    }
    SBAR();
    lds_pub(myp, (unsigned)(2 * t + 2));
    refill(2 * t + 1);
  }
  const __amdgpu_buffer_rsrc_t rO = __builtin_amdgcn_make_buffer_rsrc((void*)op, 0, 2 * 131072, 0x00020000);
#pragma unroll
  for (int u = 0; u < QT; ++u) {
    const float inv = 1.0f / lacc[u][0];
#pragma unroll
    for (int s2 = 0; s2 < 4; ++s2)
      __builtin_amdgcn_raw_buffer_store_b128(pack_pair(o[2 * s2][u] * inv, o[2 * s2 + 1][u] * inv), rO, lane * 16 + u * 131072 + s2 * 1024, 0, 16);
  }
  WAIT_VM(0);
  if (lane == 0) {
    const unsigned old = __hip_atomic_fetch_add(reinterpret_cast<LAS unsigned*>(lds0 + L_FLAG + 192u + 4u * (unsigned)hh), 1u, __ATOMIC_RELAXED, __HIP_MEMORY_SCOPE_WORKGROUP);
    if (old == 3u) __hip_atomic_store(c.oflag, get_tag(lds0), __ATOMIC_RELAXED, __HIP_MEMORY_SCOPE_AGENT);
  }
}

DI void q_stage0(const K1Ctx& c) {
  const int lane = c.lane, wave = c.wave;
  const int g = lane >> 4, qq = (lane >> 2) & 3, p = lane & 3;
  const unsigned r8 = (lane >> 2) & 7;
  const __amdgpu_buffer_rsrc_t rX = __builtin_amdgcn_make_buffer_rsrc((void*)c.xg, 0, SQ * DM * 2, 0x00020000);
  const __amdgpu_buffer_rsrc_t rW = __builtin_amdgcn_make_buffer_rsrc((void*)c.wg, 0, (unsigned)((size_t)DM * HD * 2 - (size_t)c.hp * 512), 0x00020000);
  const unsigned vw = (qq * 8192 + g * 64 + p * 16) ^ ((wave & 1) ? 32u : 0u);
  const unsigned vx = (r8 * 2048 + (lane >> 5) * 64 + p * 16) ^ ((wave & 1) ? 32u : 0u);
  const unsigned so = (unsigned)(64 * c.rot + 8 * wave) * 8192u, ld = c.lds0 + (unsigned)wave * 4096u, sx = (unsigned)c.rot * 128u;
  dma16d(rW, vw, so, ld); dma16d(rW, vw, so + 256u, ld + 1024u);
  dma16d(rW, vw, so + 4u * 8192u, ld + 2048u); dma16d(rW, vw, so + 4u * 8192u + 256u, ld + 3072u);
  dma16d(rX, vx, sx + (unsigned)wave * 16384u, c.lds0 + 32768u + (unsigned)wave * 1024u);
  if (wave < 6) dma16d(rX, vx, sx + (unsigned)(wave + 8) * 16384u, c.lds0 + 32768u + (unsigned)(wave + 8) * 1024u);
}

template <int QT>
DI void phase_q(const K1Ctx& c, f32x4 (&acc)[8][QT]) {
  const int lane = c.lane, wave = c.wave, hh = c.hh, w3 = c.w3;
  const unsigned lds0 = c.lds0;
  char* ows = c.ows; (void)ows;
  const int i = lane & 15, g = lane >> 4, qq = (lane >> 2) & 3, p = lane & 3;
  const unsigned wb = lds0 + hh * 1024 + g * 4096 + qq * 64 + p * 8;
  const unsigned wE0 = wb + ((g & 1) * 32), wO0 = wb + (((g & 1) ^ 1) * 32);
  const unsigned xa0 = lds0 + 32768 + w3 * 4096 + (i >> 3) * 1024 + (i & 7) * 64 + ((g ^ ((i >> 3) << 1)) * 16);
  const unsigned aLD = lds0 + L_FLAG + 64, aRD = lds0 + L_FLAG + 96;
  const unsigned gLD = aLD + 4u * (unsigned)(lane & 1);
  const unsigned aPR = aRD + 4u * (unsigned)(wave ^ 4);
#pragma unroll
  for (int t = 0; t < 8; ++t)
#pragma unroll
    for (int u = 0; u < QT; ++u) acc[t][u] = f32x4{0.f, 0.f, 0.f, 0.f};
  bf16x8 a0[8], b0[QT], a1[8], b1[QT];
  unsigned tj = 0; (void)tj;
  q_stage0(c);
  WAIT_VM(0);
  BAR();
  STAMP(2);
  q_read<QT>(a0, b0, wE0, wO0, xa0);
  __builtin_amdgcn_s_waitcnt(0xC07F);
  SBAR();
  q_read<QT>(a1, b1, wE0 + 16384, wO0 + 16384, xa0 + 512);
  q_mma<QT>(acc, a0, b0);
  q_sched<QT>();
  SBAR();
  __builtin_amdgcn_s_waitcnt(0xC07F);
  lds_pub(aRD + 4u * (unsigned)wave, 1u);
  lds_wait_ge(gLD, 2u);
  __builtin_amdgcn_s_waitcnt(0xC07F);
  unsigned ds = DSZ;
  for (int j = 1; j < 16; ++j) {
    SBAR();
    q_read<QT>(a0, b0, wE0 + ds, wO0 + ds, xa0 + ds);
    q_mma<QT>(acc, a1, b1);
    q_sched<QT>();
    SBAR();
    __builtin_amdgcn_s_waitcnt(0xC07F);
    SBAR();
    q_read<QT>(a1, b1, wE0 + ds + 16384, wO0 + ds + 16384, xa0 + ds + 512);
    const unsigned fld = *reinterpret_cast<volatile LAS unsigned*>(gLD);
    unsigned fpr = 0;
    if (QT == 2 && K1_DYNP) fpr = *reinterpret_cast<volatile LAS unsigned*>(aPR);
    q_mma<QT>(acc, a0, b0);
    q_sched<QT>();
    SBAR();
    __builtin_amdgcn_s_waitcnt(0xC07F);
    lds_pub(aRD + 4u * (unsigned)wave, (unsigned)(j + 1));
    if (QT == 2 && K1_DYNP) {
      if (rfl(fpr) >= (unsigned)(j + 1)) __builtin_amdgcn_s_setprio(1); else __builtin_amdgcn_s_setprio(0);
    }
    const unsigned need = (unsigned)(j < 15 ? j + 2 : 16);
    if (!__all(fld >= need)) {
      lds_wait_ge(gLD, need);
      if (QT == 2 && K1_DYNP == 2) __builtin_amdgcn_s_setprio(1);
    }
    ds = (ds == 2 * DSZ) ? 0u : ds + DSZ;
  }
  q_mma<QT>(acc, a1, b1);
  __builtin_amdgcn_s_setprio(0);
}

DI void phase_q_loader(const K1Ctx& c, unsigned vk0, unsigned vv) {
  const int lane = c.lane, hh = c.hh;
  const unsigned lds0 = c.lds0;
  char* ows = c.ows; (void)ows;
  const int g = lane >> 4, qq = (lane >> 2) & 3, p = lane & 3;
  const unsigned r8 = (lane >> 2) & 7;
  const unsigned aLD = lds0 + L_FLAG + 64 + 4u * (unsigned)hh, aRD = lds0 + L_FLAG + 96;
  const int rl = lane & 7;
  const unsigned gRD = aRD + 4u * (unsigned)(rl == 3 ? 0 : (rl == 6 ? 7 : rl));
  const __amdgpu_buffer_rsrc_t rX = __builtin_amdgcn_make_buffer_rsrc((void*)c.xg, 0, SQ * DM * 2, 0x00020000);
  const __amdgpu_buffer_rsrc_t rW = __builtin_amdgcn_make_buffer_rsrc((void*)(c.wg - 4096), 0, (unsigned)((size_t)DM * HD * 2 - (size_t)c.hp * 512) + 4096u, 0x00020000);
  const __amdgpu_buffer_rsrc_t rK = __builtin_amdgcn_make_buffer_rsrc((void*)c.kg, 0, SK * DH * 2, 0x00020000);
  const __amdgpu_buffer_rsrc_t rV = __builtin_amdgcn_make_buffer_rsrc((void*)c.vg, 0, SK * DH * 2, 0x00020000);
  const unsigned vwa = qq * 8192 + g * 64 + p * 16 + 4096u, vwb = (qq * 8192 + g * 64 + (p ^ 2) * 16) + 4096u;
  const unsigned vxa = (r8 * 2048 + (lane >> 5) * 64 + p * 16) ^ (hh ? 32u : 0u), vxb = vxa ^ 32u;
  const unsigned vk1 = vk0 ^ 32u;
  constexpr unsigned D1 = 256u - 1024u, D2 = 4u * 8192u - 2048u, D3 = 4u * 8192u + 256u - 3072u, DX = 16384u - 1024u;
  const unsigned wa1 = vwa + D1, wa2 = vwa + D2, wa3 = vwa + D3, wb1 = vwb + D1, wb2 = vwb + D2, wb3 = vwb + D3;
  const unsigned x1 = vxb + DX, x2 = vxa + 2 * DX, x3 = vxb + 3 * DX;
  auto issueA = [&](int j, unsigned ds) {
    j = (j & ~(K1_WIN - 1)) | ((j + c.rot) & (K1_WIN - 1));
    const unsigned so = (unsigned)(64 * j + 32 * hh) * 8192u, ld = lds0 + ds + (unsigned)hh * 16384u;
    dma16x4(rW, vwa, wa1, wa2, wa3, so, ld);
    dma16x4(rW, vwb, wb1, wb2, wb3, so + 8u * 8192u, ld + 4096u);
    dma16x4(rW, vwa, wa1, wa2, wa3, so + 16u * 8192u, ld + 8192u);
  };
  auto issueB = [&](int j, unsigned ds) {
    j = (j & ~(K1_WIN - 1)) | ((j + c.rot) & (K1_WIN - 1));
    const unsigned so = (unsigned)(64 * j + 32 * hh) * 8192u, ld = lds0 + ds + (unsigned)hh * 16384u;
    dma16x4(rW, vwb, wb1, wb2, wb3, so + 24u * 8192u, ld + 12288u);
    const unsigned sx = (unsigned)j * 128u + (unsigned)(7 * hh) * 16384u, lx = lds0 + ds + 32768u + (unsigned)(7 * hh) * 1024u;
    dma16x4(rX, vxa, x1, x2, x3, sx, lx);
    dma16x3(rX, vxa, x1, x2, sx + 4u * 16384u, lx + 4096u);
  };
  const int bd = c.bd;
  const int kt0 = c.krot * 16384, kt1 = ((c.krot + 1) & 7) * 16384;
  u32x4 kv[48];
  __builtin_amdgcn_s_setprio(K1_LPRIO);
  q_stage0(c);
  if (hh == 0) reinterpret_cast<volatile LAS unsigned*>(lds0 + L_FLAG)[lane] = 0u;
  WAIT_VM(0);
  BAR();
  const __amdgpu_buffer_rsrc_t rS = __builtin_amdgcn_make_buffer_rsrc((void*)SEQ, 0, 256, 0x00020000);
  issueA(1, DSZ); issueB(1, DSZ);
  dma_flag(rS, 2u, aLD);
  unsigned dn = 2 * DSZ;
  int j = 0;
  unsigned ta = 0, tb = 0, tc = 0; (void)ta; (void)tb; (void)tc;
#define LT(v) do {} while (0)
  auto run = [&](int jend) {
#pragma clang loop unroll(disable)
    for (; j <= jend; ++j) {
      LT(ta);
      if (j >= 1) lds_wait_ge(gRD, (unsigned)j);
      LT(tb);
      issueA(j + 2, dn);
      issueB(j + 2, dn);
      dma_flag(rS, (unsigned)(j + 3), aLD);
      LT(tc);
      dn = (dn == 2 * DSZ) ? 0u : dn + DSZ;
    }
  };
  run(K1_JB0 + bd);
#pragma unroll
  for (int k = 0; k < 16; ++k) kv[k] = __builtin_amdgcn_raw_buffer_load_b128(rK, (k & 2) ? vk1 : vk0, kt0 + (k >> 1) * 2048 + (k & 1) * 128, KVAUX);
  run(K1_JB1 + bd);
#pragma unroll
  for (int k = 0; k < 16; ++k) kv[16 + k] = __builtin_amdgcn_raw_buffer_load_b128(rV, vv, kt0 + (k >> 1) * 2048 + (k & 1) * 128, KVAUX);
  run(K1_JB2 + bd);
#pragma unroll
  for (int k = 0; k < 16; ++k) kv[32 + k] = __builtin_amdgcn_raw_buffer_load_b128(rK, (k & 2) ? vk1 : vk0, kt1 + (k >> 1) * 2048 + (k & 1) * 128, KVAUX);
  run(13);
  if (hh == 0) {
    const unsigned long long e = __hip_atomic_load(c.epoch, __ATOMIC_RELAXED, __HIP_MEMORY_SCOPE_AGENT);
    *reinterpret_cast<volatile LAS unsigned long long*>(lds0 + L_TAG) = e + 1ull;
  }
  lds_wait_ge(gRD, 14u);
  {
    const unsigned a = lds0 + ring_slot(hh, 0) + lane * 16;
#pragma unroll
    for (int k = 0; k < 16; ++k) *reinterpret_cast<LAS u32x4*>(a + k * 1024) = kv[k];
  }
  lds_wait_ge(gRD, 15u);
  {
    const unsigned a1 = lds0 + ring_slot(hh, 1) + lane * 16, a2 = lds0 + ring_slot(hh, 2) + lane * 16;
#pragma unroll
    for (int k = 0; k < 16; ++k) *reinterpret_cast<LAS u32x4*>(a1 + k * 1024) = kv[16 + k];
#pragma unroll
    for (int k = 0; k < 16; ++k) *reinterpret_cast<LAS u32x4*>(a2 + k * 1024) = kv[32 + k];
  }
  __builtin_amdgcn_s_setprio(0);
}

#define K2_WNT 0
DI unsigned oslot(int n) { return (unsigned)((0x287410653ull >> (4 * n)) & 15ull) * SLOT; }
DI bool poll4(gu64* f, unsigned long long tag, int lane) {
  for (int it = 0; it < (1 << 20); ++it) {
    const unsigned long long v = __hip_atomic_load(f + (lane & 3), __ATOMIC_RELAXED, __HIP_MEMORY_SCOPE_AGENT);
    if (__all(v == tag)) return true;
    __builtin_amdgcn_s_sleep(2);
  }
  return false;
}

__global__ __launch_bounds__(512, 2) __attribute__((amdgpu_waves_per_eu(2, 2))) void k1_qattn(
    const __hip_bfloat16* __restrict__ xA, const __hip_bfloat16* __restrict__ kA, const __hip_bfloat16* __restrict__ vA,
    const __hip_bfloat16* __restrict__ wqA, const __hip_bfloat16* __restrict__ woA, const __hip_bfloat16* __restrict__ xB,
    const __hip_bfloat16* __restrict__ kB, const __hip_bfloat16* __restrict__ vB, const __hip_bfloat16* __restrict__ wqB,
    const __hip_bfloat16* __restrict__ woB, __hip_bfloat16* __restrict__ ows, __hip_bfloat16* __restrict__ out) {
  extern __shared__ __attribute__((aligned(16))) char shm[];
  const unsigned lds0 = (unsigned)(uintptr_t)(LAS char*)shm;
  const int tid = threadIdx.x, lane = tid & 63;
  const int wave = (int)rfl((unsigned)tid >> 6);
  const int hh = wave >> 2, w3 = (wave & 3) ^ ((wave >= 6) ? 1 : 0);
  const int bid = blockIdx.x, xcd = bid & 7, jj = bid >> 3;
  const int side = xcd >> 2, kq = xcd & 3, b = jj & 7, hpl = jj >> 3, hp = kq * 4 + hpl;
  const int bp = b >> 1, nt = (b & 1) * 4 + hpl, grp = side * 4 + bp;
  gu64* epoch = (gu64*)((char*)ows + WS_FLAG + (size_t)grp * 128);
  gu64* oflags = (gu64*)((char*)ows + WS_FLAG + 4096 + (size_t)(grp * 16) * 128);
  gu64* pflags = (gu64*)((char*)ows + WS_FLAG + 4096 + 16384 + (size_t)(grp * 8 + nt) * 128);
  K1Ctx c;
  c.xg = (const char*)(side ? xB : xA) + (size_t)b * SQ * DM * 2;
  c.wg = (const char*)(side ? wqB : wqA) + (size_t)hp * 512;
  c.kg = (const char*)(side ? kB : kA) + (size_t)(b * NH + 2 * hp + hh) * SK * DH * 2;
  c.vg = (const char*)(side ? vB : vA) + (size_t)(b * NH + 2 * hp + hh) * SK * DH * 2;
  c.lds0 = lds0; c.wave = wave; c.hh = hh; c.w3 = w3; c.lane = lane; c.b = b; c.hpl = hpl; c.hp = hp; c.jj = jj; c.ows = (char*)ows;
  c.bd = ((jj >> 2) & 1) * K1_JBD;
  c.rot = (K1_WIN == 4) ? ((b + hpl) & 3) : (K1_WIN == 8) ? ((b + 2 * hpl) & 7) : (K1_WIN == 2) ? ((b + hpl) & 1) : 0;
#define K1_KROT 1
  c.krot = K1_KROT ? ((jj + xcd + 4 * hh + (jj >> 3)) & 7) : 0;
  c.oflag = oflags + hp * 16 + (b & 1) * 2 + hh;
  const unsigned r8 = (lane >> 2) & 7;
  const unsigned vk0 = r8 * 256 + (lane >> 5) * 64 + (lane & 3) * 16;
  const unsigned vv = r8 * 256 + (lane >> 5) * 64 + ((lane & 3) ^ ((r8 >> 2) << 1)) * 16;
  const char* op = (const char*)ows + ((size_t)((side * NB + b) * 7 + 2 * w3) * 128 + (2 * hp + hh) * 4) * 1024;
  const unsigned qx = lds0 + L_QX + (unsigned)hh * 4096u + lane * 16;
  STAMP(0); STAMPC(8);
  c.epoch = epoch;
  if (w3 < 3) {
    bf16x8 qf[2][4];
    if (w3 < 2) {
      f32x4 acc[8][2];
      phase_q<2>(c, acc);
#pragma unroll
      for (int s = 0; s < 4; ++s)
#pragma unroll
        for (int u = 0; u < 2; ++u) qf[u][s] = __builtin_bit_cast(bf16x8, pack_pair(acc[2 * s][u] * SM_SC, acc[2 * s + 1][u] * SM_SC));
    } else {
      f32x4 acc[8][3];
      phase_q<3>(c, acc);
#pragma unroll
      for (int s = 0; s < 4; ++s) {
#pragma unroll
        for (int u = 0; u < 2; ++u) qf[u][s] = __builtin_bit_cast(bf16x8, pack_pair(acc[2 * s][u] * SM_SC, acc[2 * s + 1][u] * SM_SC));
        *reinterpret_cast<LAS u32x4*>(qx + s * 1024) = pack_pair(acc[2 * s][2] * SM_SC, acc[2 * s + 1][2] * SM_SC);
      }
    }
    BAR();
    STAMP(1); STAMPC(9);
    if (hh == 0) phase_a<2, 5>(c, qf, op, vk0, vv);
    else phase_a<2, 4>(c, qf, op, vk0, vv);
  } else {
    phase_q_loader(c, vk0, vv);
    BAR();
    bf16x8 qf[1][4];
#pragma unroll
    for (int s = 0; s < 4; ++s) qf[0][s] = lds_b128(qx + s * 1024);
    if (hh == 0) phase_a<1, 5>(c, qf, op, vk0, vv);
    else phase_a<1, 4>(c, qf, op, vk0, vv);
  }
  STAMP(6); STAMPC(11);

  const int i = lane & 15, g = lane >> 4, qq = (lane >> 2) & 3, p = lane & 3;
  const unsigned aFULL = lds0 + L_FLAG + 128, aREADY = lds0 + L_FLAG + 132, aPROG = lds0 + L_FLAG + 160;
  const int tile = grp * 8 + nt;
  const char* pb = (const char*)ows + WS_PART + (size_t)tile * (16 * 7 * 4096);
  const __amdgpu_buffer_rsrc_t rP = __builtin_amdgcn_make_buffer_rsrc((void*)pb, 0, 16 * 7 * 4096, 0x00020000);
  const int u = wave - (wave > 3 ? 1 : 0);
  STAMP2(0);
  f32x4 acc[8][2];
  if (wave == 3) {
    __builtin_amdgcn_s_setprio(3);
    const unsigned long long tag = get_tag(lds0);
    const char* wg = (const char*)(side ? woB : woA) + (size_t)kq * 1024 * 2048 + (size_t)nt * 256;
    const __amdgpu_buffer_rsrc_t rW = __builtin_amdgcn_make_buffer_rsrc((void*)wg, 0, (unsigned)(1024 * 2048 - nt * 256), 0x00020000);
    const __amdgpu_buffer_rsrc_t rS = __builtin_amdgcn_make_buffer_rsrc((void*)SEQ, 0, 256, 0x00020000);
    constexpr unsigned DW = 4u * 2048u - 1024u;
    const unsigned vw0 = qq * 2048 + g * 64 + p * 16, vw1 = vw0 + DW, vw2 = (vw0 ^ 32u) + 2 * DW, vw3 = (vw0 ^ 32u) + 3 * DW;
    auto issue = [&](int n, int pos) {
      const unsigned so = (unsigned)((((hpl + (n >> 2)) & 3) * 4 + (n & 3)) * 64) * 2048u, ld = lds0 + oslot(pos);
#pragma unroll
      for (int k = 0; k < 4; ++k) dma16x4(rW, vw0, vw1, vw2, vw3, so + (unsigned)k * 16u * 2048u, ld + (unsigned)k * 4096u);
      dma_flag(rS, (unsigned)(n + 1), aFULL);
    };
    const unsigned gA0 = lds0 + L_FLAG + 32u + 4u * (unsigned)(lane & 3), gA1 = gA0 + 16u;
    lds_wait_ge(gA0, 16u);
    for (int n = 0; n < 5; ++n) issue(n, n);
    (void)poll4(oflags + (kq * 4 + hpl) * 16, tag, lane);
    lds_pub(aREADY, 1u);
    lds_wait_ge(gA1, 16u);
    for (int n = 5; n < 9; ++n) issue(n, n);
    const unsigned gP = aPROG + 4u * (unsigned)((lane & 7) < 7 ? (lane & 7) : 6);
    int n = 9;
    for (int ch = 1; ch < 4; ++ch) {
      (void)poll4(oflags + (kq * 4 + ((hpl + ch) & 3)) * 16, tag, lane);
      lds_pub(aREADY, (unsigned)(ch + 1));
      const int nend = (ch == 3) ? 16 : 9 + 3 * ch;
      for (; n < nend; ++n) {
        lds_wait_ge(gP, (unsigned)(n - 8));
        issue(n, n - 9);
      }
    }
    __builtin_amdgcn_s_setprio(0);
  } else {
    const char* og = (const char*)ows + ((size_t)((side * NB + 2 * bp) * 7 + u) * 128 + kq * 32) * 1024;
    const __amdgpu_buffer_rsrc_t rO0 = __builtin_amdgcn_make_buffer_rsrc((void*)og, 0, 32768, 0x00020000);
    const __amdgpu_buffer_rsrc_t rO1 = __builtin_amdgcn_make_buffer_rsrc((void*)(og + 7 * 131072), 0, 32768, 0x00020000);
    const unsigned wb = lds0 + g * 2048 + qq * 64 + p * 8;
    const unsigned wE = wb + (g & 1) * 32, wO = wb + ((g & 1) ^ 1) * 32;
    u32x4 of[8][2];
    lds_wait_ge(aREADY, 1u);
#pragma unroll
    for (int k = 0; k < 8; ++k) {
      of[k][0] = __builtin_amdgcn_raw_buffer_load_b128(rO0, lane * 16, (hpl * 8 + k) * 1024, 16);
      of[k][1] = __builtin_amdgcn_raw_buffer_load_b128(rO1, lane * 16, (hpl * 8 + k) * 1024, 16);
    }
#pragma unroll
    for (int t = 0; t < 8; ++t) { acc[t][0] = f32x4{0.f, 0.f, 0.f, 0.f}; acc[t][1] = f32x4{0.f, 0.f, 0.f, 0.f}; }
    bf16x8 af[2][8];
    auto readA = [&](bf16x8 (&a)[8], unsigned sl) {
      unsigned aE = wE + sl, aO = wO + sl;
      asm volatile("" : "+v"(aE), "+v"(aO));
#pragma unroll
      for (int t = 0; t < 8; ++t) {
        const unsigned ad = ((t & 1) ? aO : aE) + (t >> 1) * 256;
        a[t] = cat8(lds_tr(ad), lds_tr(ad + 1024));
      }
    };
    lds_wait_ge(aFULL, 1u);
    int pos = 0;
    unsigned slot = oslot(0);
    readA(af[0], slot);
    for (int ch = 0; ch < 4; ++ch) {
      if (ch < 3) lds_wait_ge(aREADY, (unsigned)(ch + 2));
      const unsigned nb = (ch < 3) ? (unsigned)(((hpl + ch + 1) & 3) * 8) * 1024u : 0x40000000u;
#pragma unroll
      for (int rr = 0; rr < 4; ++rr) {
        const int st = ch * 4 + rr;
        pos = (pos == 8) ? 0 : pos + 1;
        const unsigned nslot = oslot(pos);
        const unsigned need = (unsigned)(st + 2 > 16 ? 16 : st + 2);
        unsigned fv = 0;
#pragma unroll
        for (int hs = 0; hs < 2; ++hs) {
          __builtin_amdgcn_s_waitcnt(0xC07F);
          if (hs == 1) lds_pub(aPROG + 4u * (unsigned)u, (unsigned)(st + 1));
          SBAR();
          if (hs == 0) fv = *reinterpret_cast<volatile LAS unsigned*>(aFULL);
          readA(af[hs ^ 1], hs == 0 ? slot + 8192u : nslot);
          const int k = rr * 2 + hs;
#pragma unroll
          for (int t = 0; t < 8; ++t) {
            acc[t][0] = mfma16(af[hs][t], __builtin_bit_cast(bf16x8, of[k][0]), acc[t][0]);
            acc[t][1] = mfma16(af[hs][t], __builtin_bit_cast(bf16x8, of[k][1]), acc[t][1]);
          }
          of[k][0] = __builtin_amdgcn_raw_buffer_load_b128(rO0, lane * 16, nb + k * 1024, 16);
          of[k][1] = __builtin_amdgcn_raw_buffer_load_b128(rO1, lane * 16, nb + k * 1024, 16);
          if (hs == 0) __builtin_amdgcn_sched_group_barrier(0x100, 1, 0);
#pragma unroll
          for (int k2 = 0; k2 < 8; ++k2) {
            __builtin_amdgcn_sched_group_barrier(0x008, 1, 0);
            __builtin_amdgcn_sched_group_barrier(0x100, 2, 0);
          }
          __builtin_amdgcn_sched_group_barrier(0x008, 8, 0);
          __builtin_amdgcn_sched_group_barrier(0x020, 2, 0);
          SBAR();
          if (hs == 0 && rfl(fv) < need) lds_wait_ge(aFULL, need);
        }
        slot = nslot;
      }
    }
    STAMP2(1);
#pragma unroll
    for (int t = 0; t < 4; ++t) {
      if (t != kq) {
        const unsigned off = (unsigned)(((t * 4 + kq) * 7 + u) * 4096);
#pragma unroll
        for (int e = 0; e < 4; ++e)
          __builtin_amdgcn_raw_buffer_store_b128(__builtin_bit_cast(u32x4, acc[2 * t + (e >> 1)][e & 1]), rP, lane * 16 + off + e * 1024, 0, 16);
      }
    }
  }
  WAIT_VM(0);
  BAR();
  if (wave == 3) {
    const unsigned long long tag = get_tag(lds0);
    if (lane == 0) __hip_atomic_store(pflags + kq, tag, __ATOMIC_RELAXED, __HIP_MEMORY_SCOPE_AGENT);
    (void)poll4(pflags, tag, lane);
    if (lane == 0) __hip_atomic_store(epoch, tag, __ATOMIC_RELAXED, __HIP_MEMORY_SCOPE_AGENT);
    asm volatile("" ::: "memory");
  }
  BAR();
  STAMP2(3);
  if (wave != 3) {
    f32x4 sum[4];
#pragma unroll
    for (int e = 0; e < 4; ++e)
      sum[e] = (kq == 0) ? acc[0 + (e >> 1)][e & 1] : (kq == 1) ? acc[2 + (e >> 1)][e & 1] : (kq == 2) ? acc[4 + (e >> 1)][e & 1] : acc[6 + (e >> 1)][e & 1];
    u32x4 pv[3][4];
#pragma unroll
    for (int k = 0; k < 3; ++k) {
      const int src = (kq + 1 + k) & 3;
      const unsigned off = (unsigned)(((kq * 4 + src) * 7 + u) * 4096);
#pragma unroll
      for (int e = 0; e < 4; ++e) pv[k][e] = __builtin_amdgcn_raw_buffer_load_b128(rP, lane * 16, off + e * 1024, 16);
    }
#pragma unroll
    for (int k = 0; k < 3; ++k)
#pragma unroll
      for (int e = 0; e < 4; ++e) sum[e] += __builtin_bit_cast(f32x4, pv[k][e]);
    char* dp = (char*)out + ((size_t)(2 * bp * SQ + 16 * u + i) * (2 * DM) + side * DM + nt * 128 + kq * 32 + 8 * g) * 2;
    *reinterpret_cast<u32x4*>(dp) = pack_pair(sum[0], sum[2]);
    *reinterpret_cast<u32x4*>(dp + (size_t)SQ * (2 * DM) * 2) = pack_pair(sum[1], sum[3]);
  }
  STAMP2(2);
}

extern "C" void kernel_launch(void* const* d_in, const int* in_sizes, int n_in, void* d_out, int out_size, void* d_ws, size_t ws_size,
                              hipStream_t stream) {
  (void)in_sizes; (void)n_in; (void)out_size; (void)ws_size;
  typedef const __hip_bfloat16* cp;
  (void)hipFuncSetAttribute((const void*)k1_qattn, hipFuncAttributeMaxDynamicSharedMemorySize, (int)K1_LDS);
  k1_qattn<<<256, 512, K1_LDS, stream>>>((cp)d_in[0], (cp)d_in[1], (cp)d_in[2], (cp)d_in[3], (cp)d_in[4], (cp)d_in[5], (cp)d_in[6], (cp)d_in[7],
                                         (cp)d_in[8], (cp)d_in[9], (__hip_bfloat16*)d_ws, (__hip_bfloat16*)d_out);
}
```
